```python
import torch

def _softmax(x, axis=-1):
    return torch.softmax(x, dim=axis)
def _gelu(x):
    return torch.nn.functional.gelu(x, approximate="tanh")
def _silu(x):
    return torch.nn.functional.silu(x)
def _sigmoid(x):
    return torch.sigmoid(x)
def _layernorm(x, axis=-1, eps=1e-5):
    m = x.mean(dim=axis, keepdim=True)
    v = x.var(dim=axis, keepdim=True, unbiased=False)
    return (x - m) / torch.sqrt(v + eps)
def _scatter_add(tgt, idx, src, axis=0):
    return tgt.clone().index_add_(axis, idx, src)
def _segment_sum(src, seg, n):
    out = torch.zeros((n,) + src.shape[1:], dtype=src.dtype)
    return out.index_add_(0, seg, src)
def _pad(x, pw):
    flat = tuple(v for pair in reversed(pw) for v in pair)
    return torch.nn.functional.pad(x, flat)

kernel_name = 'standalone__kv_cache_quant_int4_int2_kv_with_per_channel_or_per_token_scales__prompt'

def setup_inputs():

    _w = (lambda a: (lambda t: t.to(torch.bfloat16) if t.is_floating_point() else t)(torch.as_tensor(a)))
    B = 1
    Sq = 2048
    Skv = 2048
    Hq = 32
    Hkv = 8
    Dh = 128
    bits = 8
    G = 128
    if G <= 0 or G >= Skv:
        G = max(1, Skv // 2)
    Skv = (Skv // G) * G
    ngk = Skv // G
    qmax = (1 << bits) - 1
    return {
        "q": _w((lambda *s: torch.randn(*s, dtype=torch.bfloat16) * 0.2)(B, Hq, Sq, Dh)),
        "qk": _w(torch.randint(0, qmax + 1, (B, Hkv, Skv, Dh)).to(torch.int32)),
        "qv": _w(torch.randint(0, qmax + 1, (B, Hkv, Skv, Dh)).to(torch.int32)),
        "k_scales": _w(torch.abs((lambda *s: torch.randn(*s, dtype=torch.bfloat16) * 0.2)(B, Hkv, ngk, Dh)) * 0.05 + 0.02),
        "k_zeros": _w((lambda *s: torch.randn(*s, dtype=torch.bfloat16) * 0.2)(B, Hkv, ngk, Dh) + float(qmax) / 2.0),
        "v_scales": _w(torch.abs((lambda *s: torch.randn(*s, dtype=torch.bfloat16) * 0.05)(B, Hkv, Skv, 1)) * 0.05 + 0.02),
        "v_zeros": _w((lambda *s: torch.randn(*s, dtype=torch.bfloat16) * 0.05)(B, Hkv, Skv, 1) + float(qmax) / 2.0),
    }

def reference(q, qk, qv, k_scales, k_zeros, v_scales, v_zeros):
    B, Hq, Sq, Dh = q.shape
    Hkv, Skv = qk.shape[1], qk.shape[2]
    ngk = k_scales.shape[2]
    G = Skv // ngk
    ks = (lambda x, n, axis: torch.repeat_interleave(x, n, dim=axis))(k_scales, G, axis=2)
    kz = (lambda x, n, axis: torch.repeat_interleave(x, n, dim=axis))(k_zeros, G, axis=2)
    k = ((lambda x, dt: x.to(dt))(qk, q.dtype) - kz) * ks
    v = ((lambda x, dt: x.to(dt))(qv, q.dtype) - v_zeros) * v_scales
    rep = Hq // Hkv
    if rep > 1:
        k = (lambda x, n, axis: torch.repeat_interleave(x, n, dim=axis))(k, rep, axis=1)
        v = (lambda x, n, axis: torch.repeat_interleave(x, n, dim=axis))(v, rep, axis=1)
    scale = float(Dh) ** -0.5
    logits = torch.einsum("bhqd,bhkd->bhqk", q, k) * scale
    p = _softmax(logits, axis=-1)
    o = torch.einsum("bhqk,bhkd->bhqd", p, v)
    return o
```

```cpp
#include <hip/hip_runtime.h>
typedef float    f32x4 __attribute__((ext_vector_type(4)));
typedef float    f32x2 __attribute__((ext_vector_type(2)));
typedef __bf16   bf16x8 __attribute__((ext_vector_type(8)));
typedef __bf16   bf16x2 __attribute__((ext_vector_type(2)));
typedef unsigned u32x4 __attribute__((ext_vector_type(4)));
typedef int      i32x4 __attribute__((ext_vector_type(4)));
#define DI __device__ __forceinline__
constexpr int HQ = 32, HKV = 8, SQ = 2048, SKV = 2048, DH = 128, GRP = 128, NT = SKV / 64;
constexpr float CSC = 0.12751743082f;
DI unsigned cvtpk(float lo, float hi) { f32x2 v = {lo, hi}; return __builtin_bit_cast(unsigned, __builtin_convertvector(v, bf16x2)); }
DI float rbf(float x) { return __uint_as_float(cvtpk(0.f, x)); }
DI float lo16(unsigned x) { return __uint_as_float(x << 16); }
DI float hi16(unsigned x) { return __uint_as_float(x & 0xffff0000u); }

struct Raw { const int* qk; const int* qv; const unsigned short* ks; const unsigned short* kz; const unsigned short* vs; const unsigned short* vz; };
DI f32x4 exact_scores(const Raw& r, int key0, int n, int g, const bf16x8 (&qf)[4]) {
  f32x4 s = {0.f, 0.f, 0.f, 0.f};
  #pragma unroll
  for (int ds = 0; ds < 4; ++ds) {
    const int d = 32 * ds + 8 * g;
    const int* src = r.qk + (size_t)(key0 + n) * DH + d;
    const i32x4 a = *(const i32x4*)src, b = *(const i32x4*)(src + 4);
    const int so = (key0 / GRP) * DH + d;
    const u32x4 sc = *(const u32x4*)(r.ks + so), z = *(const u32x4*)(r.kz + so);
    u32x4 o;
    #pragma unroll
    for (int e = 0; e < 4; ++e) {
      const int x0 = e < 2 ? a[2 * e] : b[2 * e - 4], x1 = e < 2 ? a[2 * e + 1] : b[2 * e - 3];
      o[e] = cvtpk(rbf(rbf(rbf((float)x0) - lo16(z[e])) * lo16(sc[e])) * CSC, rbf(rbf(rbf((float)x1) - hi16(z[e])) * hi16(sc[e])) * CSC);
    }
    s = __builtin_amdgcn_mfma_f32_16x16x32_bf16(__builtin_bit_cast(bf16x8, o), qf[ds], s, 0, 0, 0);
  }
  return s;
}
DI void attn_exact(const char* qrows, const Raw& r, int kvh, char* orows) {
  const int l = __builtin_amdgcn_mbcnt_hi(~0u, __builtin_amdgcn_mbcnt_lo(~0u, 0u)), n = l & 15, g = l >> 4;
  #pragma unroll 1
  for (int qb = 0; qb < 4; ++qb) {
    bf16x8 qf[4];
    #pragma unroll
    for (int ds = 0; ds < 4; ++ds) qf[ds] = *(const bf16x8*)(qrows + (16 * qb + n) * 256 + 64 * ds + 16 * g);
    float m = -__builtin_inff();
    #pragma unroll 1
    for (int x = 0; x < SKV / 16; ++x) {
      const f32x4 s = exact_scores(r, kvh * SKV + 16 * x, n, g, qf);
      m = fmaxf(fmaxf(fmaxf(m, s[0]), fmaxf(s[1], s[2])), s[3]);
    }
    m = fmaxf(m, __shfl_xor(m, 16)); m = fmaxf(m, __shfl_xor(m, 32));
    f32x4 o[8];
    #pragma unroll
    for (int db = 0; db < 8; ++db) o[db] = f32x4{0.f, 0.f, 0.f, 0.f};
    float ls = 0.f;
    #pragma unroll 1
    for (int x = 0; x < SKV / 32; ++x) {
      const int key0 = kvh * SKV + 32 * x;
      u32x4 pk;
      #pragma unroll
      for (int hb = 0; hb < 2; ++hb) {
        const f32x4 s = exact_scores(r, key0 + 16 * hb, n, g, qf);
        float p[4];
        #pragma unroll
        for (int e = 0; e < 4; ++e) { p[e] = __builtin_amdgcn_exp2f(s[e] - m); ls += p[e]; }
        pk[2 * hb] = cvtpk(p[0], p[1]); pk[2 * hb + 1] = cvtpk(p[2], p[3]);
      }
      float y[8][8];
      #pragma unroll
      for (int j = 0; j < 8; ++j) {
        const int key = key0 + 16 * (j >> 2) + 4 * g + (j & 3);
        const int* src = r.qv + (size_t)key * DH + 8 * n;
        const i32x4 a = *(const i32x4*)src, b = *(const i32x4*)(src + 4);
        const float sc = lo16(r.vs[key]), z = lo16(r.vz[key]);
        #pragma unroll
        for (int e = 0; e < 8; ++e) y[j][e] = rbf(rbf((float)(e < 4 ? a[e] : b[e - 4])) - z) * sc;
      }
      #pragma unroll
      for (int db = 0; db < 8; ++db) {
        u32x4 vf;
        #pragma unroll
        for (int e = 0; e < 4; ++e) vf[e] = cvtpk(y[2 * e][db], y[2 * e + 1][db]);
        o[db] = __builtin_amdgcn_mfma_f32_16x16x32_bf16(__builtin_bit_cast(bf16x8, pk), __builtin_bit_cast(bf16x8, vf), o[db], 0, 0, 0);
      }
    }
    const f32x4 lt = __builtin_amdgcn_mfma_f32_16x16x4f32(ls, 1.0f, f32x4{0.f, 0.f, 0.f, 0.f}, 0, 0, 0);
    #pragma unroll
    for (int e = 0; e < 4; ++e) {
      const float il = 1.0f / lt[e];
      u32x4 wv;
      #pragma unroll
      for (int k = 0; k < 4; ++k) wv[k] = cvtpk(o[2 * k][e] * il, o[2 * k + 1][e] * il);
      *(u32x4*)(orows + (16 * qb + 4 * g + e) * 256 + 16 * n) = wv;
    }
  }
}

struct Args { const char* q; const int* qk; const int* qv; const unsigned short* ks; const unsigned short* kz; const unsigned short* vs; const unsigned short* vz; char* ws; char* out; };
typedef const Args __attribute__((address_space(4)))* ArgsPtr;
__global__ __launch_bounds__(256, 1) __attribute__((aligned(4096))) void attn_main(const char* q, const int* qk, const int* qv, const unsigned short* ks, const unsigned short* kz,
                                                    const unsigned short* vs, const unsigned short* vz, char* ws, char* out) {
  __shared__ __attribute__((aligned(16))) char lds[163840];
  const unsigned w = __builtin_amdgcn_readfirstlane(threadIdx.x >> 6), b = blockIdx.x;
  ArgsPtr ka = (ArgsPtr)__builtin_amdgcn_kernarg_segment_ptr();
  unsigned flag;
  asm volatile(
    "s_load_dwordx16 s[52:67], %[ka], 0x0\n"
    "v_mbcnt_lo_u32_b32 v8, -1, 0\n"
    "s_getpc_b64 s[40:41]\n"
    "10:\n"
    "v_mbcnt_hi_u32_b32 v8, -1, v8\n"
    "s_lshl_b32 s6, %[w], 13\n"
    "v_lshl_add_u32 v17, v8, 7, s6\n"
    "v_min_u32 v17, 11f-10b-1152, v17\n"
    "global_load_dword v14, v17, s[40:41] offset:1152\n"
    "v_and_b32 v9, 15, v8\n"
    "s_and_b32 s51, %[bid], 7\n"
    "s_lshr_b32 s87, %[bid], 3\n"
    "s_lshl_b32 s50, s51, 5\n"
    "s_add_u32 s50, s50, s87\n"
    "s_lshl_b32 s50, s50, 6\n"
    "v_lshrrev_b32 v11, 3, v8\n"
    "v_and_b32 v12, 7, v8\n"
    "s_lshl_b32 s48, s50, 2\n"
    "s_add_u32 s48, s48, %[w]\n"
    "s_lshl_b32 s48, s48, 7\n"
    "v_lshl_add_u32 v241, v11, 9, s48\n"
    "v_lshl_add_u32 v241, v12, 4, v241\n"
    "v_and_b32 v11, 24, v8\n"
    "s_and_b32 s48, s50, 0xffffff80\n"
    "s_lshl_b32 s49, %[w], 5\n"
    "s_add_u32 s48, s48, s49\n"
    "v_add_lshl_u32 v13, s48, v11, 1\n"
    "s_and_b32 s48, %[w], 1\n"
    "s_lshl_b32 s48, s48, 3\n"
    "s_add_u32 s86, s48, s50\n"
    "s_lshl_b32 s48, s86, 5\n"
    "s_lshr_b32 s49, %[w], 1\n"
    "s_add_u32 s48, s48, s49\n"
    "s_lshl_b32 s48, s48, 4\n"
    "v_lshrrev_b32 v10, 4, v8\n"
    "v_lshl_add_u32 v242, v9, 5, s48\n"
    "v_lshl_add_u32 v242, v10, 9, v242\n"
    "s_lshl_b32 s86, s86, 1\n"
    "v_mov_b32 v15, 0x104000\n"
    "s_lshl_b32 s51, s51, 21\n"
    "s_lshl_b32 s48, s50, 10\n"
    "s_lshl_b32 s35, %[w], 14\n"
    "s_add_u32 s48, s48, s35\n"
    "v_xor_b32 v84, v9, v10\n"
    "v_lshl_add_u32 v80, v10, 8, s48\n"
    "v_lshl_add_u32 v80, v84, 4, v80\n"
    "v_lshl_add_u32 v250, v9, 4, s48\n"
    "v_lshl_add_u32 v250, v10, 10, v250\n"
    "s_add_u32 s35, s35, 0x18000\n"
    "s_xor_b32 s34, s87, %[bid]\n"
    "s_and_b32 s34, s34, 3\n"
    "s_lshl_b32 s48, s34, 12\n"
    "v_xor_b32 v80, s48, v80\n"
    "v_xor_b32 v250, s48, v250\n"
    "s_lshl_b32 s48, s34, 13\n"
    "v_xor_b32 v241, s48, v241\n"
    "v_xor_b32 v242, s48, v242\n"
    "s_lshl_b32 s34, s34, 5\n"
    "v_xor_b32 v81, 64, v80\n"
    "v_xor_b32 v82, 128, v80\n"
    "v_xor_b32 v83, 192, v80\n"
    "s_waitcnt lgkmcnt(0)\n"
    "global_load_dwordx4 v[0:3], v13, s[58:59]\n"
    "global_load_dwordx4 v[4:7], v13, s[60:61]\n"
    "s_lshr_b32 s48, s6, 1\n"
    "s_add_u32 m0, s48, 0x10000\n"
    "s_nop 0\n"
    "global_load_lds_dwordx4 v241, s[54:55] offset:0\n"
    "s_add_u32 s58, s54, 0xc00\n"
    "s_addc_u32 s59, s55, 0\n"
    "global_load_lds_dwordx4 v241, s[58:59] offset:1024\n"
    "s_sub_u32 s56, s56, 0x800\n"
    "s_subb_u32 s57, s57, 0\n"
    "global_load_lds_dwordx4 v242, s[56:57] offset:2048\n"
    "s_add_u32 s84, s56, 0x400\n"
    "s_addc_u32 s85, s57, 0\n"
    "global_load_lds_dwordx4 v242, s[84:85] offset:3072\n"
    "s_add_u32 s66, s66, s51\n"
    "s_addc_u32 s67, s67, 0\n"
    "global_load_dwordx2 v[246:247], v15, s[66:67] sc1\n"
    "s_add_u32 s62, s62, s86\n"
    "s_addc_u32 s63, s63, 0\n"
    "s_add_u32 s64, s64, s86\n"
    "s_addc_u32 s65, s65, 0\n"
    "s_load_dwordx2 s[46:47], %[ka], 0x40\n"
    "s_xor_b32 s48, s34, 0\n"
    "s_load_dwordx4 s[12:15], s[62:63], s48\n"
    "s_load_dwordx4 s[20:23], s[64:65], s48\n"
    "s_xor_b32 s48, s34, 64\n"
    "s_load_dwordx4 s[28:31], s[62:63], s48\n"
    "s_load_dwordx4 s[72:75], s[64:65], s48\n"
    "s_xor_b32 s48, s34, 96\n"
    "s_load_dwordx4 s[80:83], s[62:63], s48\n"
    "s_load_dwordx4 s[92:95], s[64:65], s48\n"
    "s_xor_b32 s48, s34, 32\n"
    "s_load_dwordx4 s[36:39], s[62:63], s48\n"
    "s_load_dwordx4 s[60:63], s[64:65], s48\n"
    "v_and_b32 v16, 31, v8\n"
    "s_lshl_b32 s48, s87, 9\n"
    "s_add_u32 s48, s48, 0x100000\n"
    "v_mov_b32 v248, s48\n"
    "s_add_u32 m0, s35, 0x0\n"
    "s_nop 0\n"
    "global_load_lds_dwordx4 v80, s[52:53] offset:0 nt\n"
    "global_load_lds_dwordx4 v81, s[52:53] offset:1024 nt\n"
    "global_load_lds_dwordx4 v82, s[52:53] offset:2048 nt\n"
    "global_load_lds_dwordx4 v83, s[52:53] offset:3072 nt\n"
    "v_xor_b32 v80, 0x1000, v80\n"
    "v_xor_b32 v81, 0x1000, v81\n"
    "v_xor_b32 v82, 0x1000, v82\n"
    "v_xor_b32 v83, 0x1000, v83\n"
    "s_mov_b32 s48, 0\n"
    "s_add_u32 s48, s48, 0x100000\n"
    "v_lshl_add_u32 v245, v16, 9, s48\n"
    "s_lshr_b32 s7, s87, 2\n"
    "s_lshl_b32 s7, s7, 15\n"
    "s_and_b32 s48, s87, 1\n"
    "s_lshl_b32 s48, s48, 13\n"
    "s_add_u32 s48, s48, s7\n"
    "s_add_u32 s44, s66, s48\n"
    "s_addc_u32 s45, s67, 0\n"
    "s_and_b32 s86, s87, 2\n"
    "s_lshl_b32 s48, s86, 6\n"
    "s_lshl_b32 s49, %[w], 10\n"
    "s_add_u32 s48, s48, s49\n"
    "v_lshl_add_u32 v243, v12, 4, s48\n"
    "v_lshl_add_u32 v243, v11, 5, v243\n"
    "v_lshrrev_b32 v12, 5, v8\n"
    "v_lshl_add_u32 v243, v12, 12, v243\n"
    "s_lshr_b32 s48, s6, 1\n"
    "s_add_u32 s48, s48, 0x10000\n"
    "v_lshl_add_u32 v249, v11, 2, s48\n"
    "v_lshl_add_u32 v249, v12, 9, v249\n"
    "v_and_b32 v12, 3, v8\n"
    "v_lshl_add_u32 v249, v12, 7, v249\n"
    "v_and_b32 v12, 4, v8\n"
    "v_lshl_add_u32 v249, v12, 8, v249\n"
    "s_add_u32 s48, s48, 0x800\n"
    "v_lshl_add_u32 v251, v9, 4, s48\n"
    "v_lshl_add_u32 v251, v10, 2, v251\n"
    "s_and_b32 s48, %[w], 1\n"
    "s_add_u32 s48, s48, s86\n"
    "s_lshl_b32 s48, s48, 8\n"
    "s_and_b32 s49, %[w], 2\n"
    "s_lshl_b32 s49, s49, 11\n"
    "s_add_u32 s48, s48, s49\n"
    "s_add_u32 s48, s48, 0x4000\n"
    "v_lshl_add_u32 v244, v10, 10, s48\n"
    "v_lshl_add_u32 v244, v9, 4, v244\n"
    "s_lshl_b32 s48, %[w], 10\n"
    "s_add_u32 s48, s48, 0x14000\n"
    "v_lshl_add_u32 v13, v8, 4, s48\n"
    "s_waitcnt lgkmcnt(0)\n"
    "s_lshl_b32 s8, s12, 16\n"
    "s_and_b32 s9, s12, 0xffff0000\n"
    "s_lshl_b32 s10, s13, 16\n"
    "s_and_b32 s11, s13, 0xffff0000\n"
    "s_lshl_b32 s12, s14, 16\n"
    "s_and_b32 s13, s14, 0xffff0000\n"
    "s_lshl_b32 s14, s15, 16\n"
    "s_and_b32 s15, s15, 0xffff0000\n"
    "s_lshl_b32 s16, s20, 16\n"
    "s_and_b32 s17, s20, 0xffff0000\n"
    "s_lshl_b32 s18, s21, 16\n"
    "s_and_b32 s19, s21, 0xffff0000\n"
    "s_lshl_b32 s20, s22, 16\n"
    "s_and_b32 s21, s22, 0xffff0000\n"
    "s_lshl_b32 s22, s23, 16\n"
    "s_and_b32 s23, s23, 0xffff0000\n"
    "s_waitcnt vmcnt(9)\n"
    "v_lshlrev_b32 v228, 16, v4\n"
    "v_and_b32 v229, 0xffff0000, v4\n"
    "v_lshlrev_b32 v230, 16, v5\n"
    "v_and_b32 v231, 0xffff0000, v5\n"
    "v_lshlrev_b32 v232, 16, v6\n"
    "v_and_b32 v233, 0xffff0000, v6\n"
    "v_lshlrev_b32 v234, 16, v7\n"
    "v_and_b32 v235, 0xffff0000, v7\n"
    "v_lshlrev_b32 v220, 16, v0\n"
    "v_and_b32 v221, 0xffff0000, v0\n"
    "v_lshlrev_b32 v222, 16, v1\n"
    "v_and_b32 v223, 0xffff0000, v1\n"
    "v_lshlrev_b32 v224, 16, v2\n"
    "v_and_b32 v225, 0xffff0000, v2\n"
    "v_lshlrev_b32 v226, 16, v3\n"
    "v_and_b32 v227, 0xffff0000, v3\n"
    "v_mul_f32 v220, 0x3e0293ee, v220\n"
    "v_mul_f32 v221, 0x3e0293ee, v221\n"
    "v_mul_f32 v222, 0x3e0293ee, v222\n"
    "v_mul_f32 v223, 0x3e0293ee, v223\n"
    "v_mul_f32 v224, 0x3e0293ee, v224\n"
    "v_mul_f32 v225, 0x3e0293ee, v225\n"
    "v_mul_f32 v226, 0x3e0293ee, v226\n"
    "v_mul_f32 v227, 0x3e0293ee, v227\n"
    "v_pk_mul_f32 v[228:229], v[228:229], v[220:221] neg_lo:[1,0] neg_hi:[1,0]\n"
    "v_pk_mul_f32 v[230:231], v[230:231], v[222:223] neg_lo:[1,0] neg_hi:[1,0]\n"
    "v_pk_mul_f32 v[232:233], v[232:233], v[224:225] neg_lo:[1,0] neg_hi:[1,0]\n"
    "v_pk_mul_f32 v[234:235], v[234:235], v[226:227] neg_lo:[1,0] neg_hi:[1,0]\n"
    "s_waitcnt vmcnt(7)\n"
    "ds_read_b128 v[204:207], v249 offset:0\n"
    "ds_read_b128 v[208:211], v249 offset:16\n"
    "s_waitcnt vmcnt(5)\n"
    "ds_read2st64_b32 v[212:213], v251 offset0:0 offset1:1\n"
    "ds_read2st64_b32 v[214:215], v251 offset0:2 offset1:3\n"
    "ds_read2st64_b32 v[216:217], v251 offset0:4 offset1:5\n"
    "ds_read2st64_b32 v[218:219], v251 offset0:6 offset1:7\n"
    "s_waitcnt lgkmcnt(5)\n"
    "v_cvt_f32_i32 v204, v204\n"
    "v_cvt_f32_i32 v205, v205\n"
    "v_cvt_f32_i32 v206, v206\n"
    "v_cvt_f32_i32 v207, v207\n"
    "s_waitcnt lgkmcnt(4)\n"
    "v_cvt_f32_i32 v208, v208\n"
    "v_cvt_f32_i32 v209, v209\n"
    "v_cvt_f32_i32 v210, v210\n"
    "v_cvt_f32_i32 v211, v211\n"
    "v_pk_fma_f32 v[204:205], v[204:205], v[220:221], v[228:229]\n"
    "v_pk_fma_f32 v[206:207], v[206:207], v[222:223], v[230:231]\n"
    "v_pk_fma_f32 v[208:209], v[208:209], v[224:225], v[232:233]\n"
    "v_pk_fma_f32 v[210:211], v[210:211], v[226:227], v[234:235]\n"
    "v_cvt_pk_bf16_f32 v204, v204, v205\n"
    "v_cvt_pk_bf16_f32 v205, v206, v207\n"
    "v_cvt_pk_bf16_f32 v206, v208, v209\n"
    "v_cvt_pk_bf16_f32 v207, v210, v211\n"
    "global_store_dwordx4 v243, v[204:207], s[44:45] sc1\n"
    "s_waitcnt lgkmcnt(3)\n"
    "v_cvt_f32_i32 v212, v212\n"
    "v_cvt_f32_i32 v213, v213\n"
    "s_waitcnt lgkmcnt(2)\n"
    "v_cvt_f32_i32 v214, v214\n"
    "v_cvt_f32_i32 v215, v215\n"
    "s_waitcnt lgkmcnt(1)\n"
    "v_cvt_f32_i32 v216, v216\n"
    "v_cvt_f32_i32 v217, v217\n"
    "s_waitcnt lgkmcnt(0)\n"
    "v_cvt_f32_i32 v218, v218\n"
    "v_cvt_f32_i32 v219, v219\n"
    "v_pk_add_f32 v[212:213], v[212:213], s[16:17] neg_lo:[0,1] neg_hi:[0,1]\n"
    "v_pk_add_f32 v[214:215], v[214:215], s[18:19] neg_lo:[0,1] neg_hi:[0,1]\n"
    "v_pk_add_f32 v[216:217], v[216:217], s[20:21] neg_lo:[0,1] neg_hi:[0,1]\n"
    "v_pk_add_f32 v[218:219], v[218:219], s[22:23] neg_lo:[0,1] neg_hi:[0,1]\n"
    "v_pk_mul_f32 v[212:213], v[212:213], s[8:9]\n"
    "v_pk_mul_f32 v[214:215], v[214:215], s[10:11]\n"
    "v_pk_mul_f32 v[216:217], v[216:217], s[12:13]\n"
    "v_pk_mul_f32 v[218:219], v[218:219], s[14:15]\n"
    "v_cvt_pk_bf16_f32 v212, v212, v213\n"
    "v_cvt_pk_bf16_f32 v213, v214, v215\n"
    "v_cvt_pk_bf16_f32 v214, v216, v217\n"
    "v_cvt_pk_bf16_f32 v215, v218, v219\n"
    "global_store_dwordx4 v244, v[212:215], s[44:45] sc1\n"
    "v_xor_b32 v208, 0x80008000, v204\n"
    "v_xor_b32 v209, 0x80008000, v205\n"
    "v_xor_b32 v210, 0x80008000, v206\n"
    "v_xor_b32 v211, 0x80008000, v207\n"
    "ds_write_b128 v13, v[208:211]\n"
    "s_add_u32 s44, s44, 0x40000\n"
    "s_addc_u32 s45, s45, 0\n"
    "v_xor_b32 v241, 0x2000, v241\n"
    "v_xor_b32 v242, 0x2000, v242\n"
    "v_mov_b32 v18, 0x7f4a7c15\n"
    "v_mov_b32 v19, 0x9e3779b9\n"
    "s_mov_b32 s34, 0\n"
    "s_lshr_b32 s48, s6, 1\n"
    "s_add_u32 s48, s48, 0x10000\n"
    "s_sub_u32 s48, s35, s48\n"
    "v_add_u32 v249, s48, v249\n"
    "v_add_u32 v251, s48, v251\n"
    "v_lshl_add_u32 v84, v84, 4, s35\n"
    "v_lshl_add_u32 v84, v9, 8, v84\n"
    "v_xor_b32 v85, 64, v84\n"
    "v_xor_b32 v86, 128, v84\n"
    "v_xor_b32 v87, 192, v84\n"
    "v_lshlrev_b32 v201, 4, v8\n"
    "v_add_u32 v202, 0x1000, v201\n"
    "v_add_u32 v203, 0x10000, v201\n"
    "v_lshlrev_b32 v13, 7, v10\n"
    "v_add_u32 v13, 0x14000, v13\n"
    "s_add_u32 s42, s66, s6\n"
    "s_addc_u32 s43, s67, 0\n"
    "s_mov_b32 s7, 0\n"
    "s_mov_b32 s50, 0xffff8000\n"
    "s_mov_b32 s51, 6\n"
    "s_mov_b64 s[40:41], s[42:43]\n"
    "s_lshl_b32 s8, s36, 16\n"
    "s_and_b32 s9, s36, 0xffff0000\n"
    "s_lshl_b32 s10, s37, 16\n"
    "s_and_b32 s11, s37, 0xffff0000\n"
    "s_lshl_b32 s12, s38, 16\n"
    "s_and_b32 s13, s38, 0xffff0000\n"
    "s_lshl_b32 s14, s39, 16\n"
    "s_and_b32 s15, s39, 0xffff0000\n"
    "s_lshl_b32 s16, s60, 16\n"
    "s_and_b32 s17, s60, 0xffff0000\n"
    "s_lshl_b32 s18, s61, 16\n"
    "s_and_b32 s19, s61, 0xffff0000\n"
    "s_lshl_b32 s20, s62, 16\n"
    "s_and_b32 s21, s62, 0xffff0000\n"
    "s_lshl_b32 s22, s63, 16\n"
    "s_and_b32 s23, s63, 0xffff0000\n"
    "s_lshl_b32 s24, s28, 16\n"
    "s_and_b32 s25, s28, 0xffff0000\n"
    "s_lshl_b32 s26, s29, 16\n"
    "s_and_b32 s27, s29, 0xffff0000\n"
    "s_lshl_b32 s28, s30, 16\n"
    "s_and_b32 s29, s30, 0xffff0000\n"
    "s_lshl_b32 s30, s31, 16\n"
    "s_and_b32 s31, s31, 0xffff0000\n"
    "s_lshl_b32 s68, s72, 16\n"
    "s_and_b32 s69, s72, 0xffff0000\n"
    "s_lshl_b32 s70, s73, 16\n"
    "s_and_b32 s71, s73, 0xffff0000\n"
    "s_lshl_b32 s72, s74, 16\n"
    "s_and_b32 s73, s74, 0xffff0000\n"
    "s_lshl_b32 s74, s75, 16\n"
    "s_and_b32 s75, s75, 0xffff0000\n"
    "s_lshl_b32 s76, s80, 16\n"
    "s_and_b32 s77, s80, 0xffff0000\n"
    "s_lshl_b32 s78, s81, 16\n"
    "s_and_b32 s79, s81, 0xffff0000\n"
    "s_lshl_b32 s80, s82, 16\n"
    "s_and_b32 s81, s82, 0xffff0000\n"
    "s_lshl_b32 s82, s83, 16\n"
    "s_and_b32 s83, s83, 0xffff0000\n"
    "s_lshl_b32 s88, s92, 16\n"
    "s_and_b32 s89, s92, 0xffff0000\n"
    "s_lshl_b32 s90, s93, 16\n"
    "s_and_b32 s91, s93, 0xffff0000\n"
    "s_lshl_b32 s92, s94, 16\n"
    "s_and_b32 s93, s94, 0xffff0000\n"
    "s_lshl_b32 s94, s95, 16\n"
    "s_and_b32 s95, s95, 0xffff0000\n"
    "s_waitcnt vmcnt(6)\n"
    "v_lshl_add_u64 v[246:247], v[246:247], 0, v[18:19]\n"
    "s_waitcnt vmcnt(0)\n"
    "s_waitcnt lgkmcnt(0)\n"
    "s_barrier\n"
    "s_cmp_lg_u32 %[w], 0\n"
    "s_cbranch_scc1 21f\n"
    "global_store_dwordx2 v248, v[246:247], s[66:67] offset:0 sc1\n"
    "21:\n"
    "s_add_u32 m0, s35, 0x1000\n"
    "s_nop 0\n"
    "global_load_lds_dwordx4 v80, s[52:53] offset:0 nt\n"
    "global_load_lds_dwordx4 v81, s[52:53] offset:1024 nt\n"
    "global_load_lds_dwordx4 v82, s[52:53] offset:2048 nt\n"
    "global_load_lds_dwordx4 v83, s[52:53] offset:3072 nt\n"
    "v_xor_b32 v80, 0x3000, v80\n"
    "v_xor_b32 v81, 0x3000, v81\n"
    "v_xor_b32 v82, 0x3000, v82\n"
    "v_xor_b32 v83, 0x3000, v83\n"
    "s_add_u32 m0, s35, 0x2000\n"
    "s_nop 0\n"
    "global_load_lds_dwordx4 v80, s[52:53] offset:0 nt\n"
    "v_xor_b32 v80, 0x1000, v80\n"
    "ds_read_b128 v[64:67], v13 offset:0\n"
    "ds_read_b128 v[68:71], v13 offset:1024\n"
    "ds_read_b128 v[72:75], v13 offset:2048\n"
    "ds_read_b128 v[76:79], v13 offset:3072\n"
    "v_mov_b32 v88, 0xc2100000\n"
    "v_mov_b32 v89, 0xc2100000\n"
    "v_mov_b32 v90, 0xc2100000\n"
    "v_mov_b32 v91, 0xc2100000\n"
    "v_accvgpr_write_b32 a196, 0\n"
    "v_accvgpr_write_b32 a197, 0\n"
    "v_accvgpr_write_b32 a198, 0\n"
    "v_accvgpr_write_b32 a199, 0\n"
    "v_accvgpr_write_b32 a200, 0\n"
    "v_accvgpr_write_b32 a201, 0\n"
    "v_accvgpr_write_b32 a202, 0\n"
    "v_accvgpr_write_b32 a203, 0\n"
    "v_accvgpr_write_b32 a204, 0\n"
    "v_accvgpr_write_b32 a205, 0\n"
    "v_accvgpr_write_b32 a206, 0\n"
    "v_accvgpr_write_b32 a207, 0\n"
    "v_accvgpr_write_b32 a208, 0\n"
    "v_accvgpr_write_b32 a209, 0\n"
    "v_accvgpr_write_b32 a210, 0\n"
    "v_accvgpr_write_b32 a211, 0\n"
    "v_mov_b32 v11, 0x3f803f80\n"
    "v_cmp_gt_u32 vcc, 2, v9\n"
    "s_nop 1\n"
    "v_cndmask_b32 v11, 0, v11, vcc\n"
    "v_accvgpr_write_b32 a192, v11\n"
    "v_accvgpr_write_b32 a193, v11\n"
    "v_accvgpr_write_b32 a194, v11\n"
    "v_accvgpr_write_b32 a195, v11\n"
    "v_and_b32 v240, 1, v8\n"
    "v_mul_u32_u24 v240, 0xaaaa, v240\n"
    "v_add_u32 v240, 0x4444, v240\n"
    "v_lshl_or_b32 v240, v240, 16, v240\n"
    "ds_read_b128 a[0:3], v84 offset:0\n"
    "ds_read_b128 a[4:7], v85 offset:0\n"
    "ds_read_b128 a[8:11], v86 offset:0\n"
    "ds_read_b128 a[12:15], v87 offset:0\n"
    "s_waitcnt lgkmcnt(3)\n"
    "v_mfma_f32_16x16x32_bf16 v[176:179], v[64:67], a[0:3], v[88:91]\n"
    "s_waitcnt lgkmcnt(2)\n"
    "v_mfma_f32_16x16x32_bf16 v[176:179], v[68:71], a[4:7], v[176:179]\n"
    "s_waitcnt lgkmcnt(1)\n"
    "v_mfma_f32_16x16x32_bf16 v[176:179], v[72:75], a[8:11], v[176:179]\n"
    "s_waitcnt lgkmcnt(0)\n"
    "v_mfma_f32_16x16x32_bf16 v[176:179], v[76:79], a[12:15], v[176:179]\n"
    "s_cmp_lg_u32 %[w], 0\n"
    "s_cbranch_scc1 25f\n"
    "s_sleep 16\n"
    "global_load_dwordx2 v[252:253], v245, s[66:67] offset:0 sc1\n"
    "s_add_u32 m0, s35, 0x2000\n"
    "s_nop 0\n"
    "s_nop 0\n"
    "global_load_lds_dwordx4 v81, s[52:53] offset:1024 nt\n"
    "global_load_lds_dwordx4 v82, s[52:53] offset:2048 nt\n"
    "global_load_lds_dwordx4 v83, s[52:53] offset:3072 nt\n"
    "v_xor_b32 v81, 0x1000, v81\n"
    "v_xor_b32 v82, 0x1000, v82\n"
    "v_xor_b32 v83, 0x1000, v83\n"
    " s_waitcnt vmcnt(3)\n"
    "v_cmp_ne_u64 vcc, v[252:253], v[246:247]\n"
    "s_cbranch_vccz 24f\n"
    "s_mov_b32 s48, 20000\n"
    "23:\n"
    "global_load_dwordx2 v[252:253], v245, s[66:67] offset:0 sc1\n"
    " s_waitcnt vmcnt(0)\n"
    "v_cmp_ne_u64 vcc, v[252:253], v[246:247]\n"
    "s_cbranch_vccz 24f\n"
    "s_sleep 1\n"
    "s_sub_u32 s48, s48, 1\n"
    "s_cbranch_scc0 23b\n"
    "s_mov_b32 s34, 1\n"
    "24:\n"
    "s_cmp_lg_u32 s87, 0\n"
    "s_cbranch_scc1 22f\n"
    "s_mov_b64 exec, 1\n"
    "global_atomic_add_x2 v15, v[18:19], s[66:67] sc1\n"
    "s_mov_b64 exec, -1\n"
    "s_branch 22f\n"
    "25:\n"
    "s_mov_b64 exec, 0\n"
    "s_cmp_eq_u32 %[w], 1\n"
    "s_cbranch_scc1 200f\n"
    "s_cmp_eq_u32 %[w], 2\n"
    "s_cbranch_scc1 203f\n"
    "s_cmp_eq_u32 %[w], 3\n"
    "s_cbranch_scc1 206f\n"
    "199:\n"
    "s_mov_b64 exec, -1\n"
    "s_sleep 16\n"
    "s_add_u32 m0, s35, 0x2000\n"
    "s_nop 0\n"
    "global_load_lds_dwordx4 v81, s[52:53] offset:1024 nt\n"
    "global_load_lds_dwordx4 v82, s[52:53] offset:2048 nt\n"
    "global_load_lds_dwordx4 v83, s[52:53] offset:3072 nt\n"
    "v_xor_b32 v81, 0x1000, v81\n"
    "v_xor_b32 v82, 0x1000, v82\n"
    "v_xor_b32 v83, 0x1000, v83\n"
    "22:\n"
    "s_barrier\n"
    "s_lshr_b32 s86, s6, 1\n"
    "s_sub_u32 s48, s40, s86\n"
    "s_subb_u32 s49, s41, 0\n"
    "s_mov_b32 m0, s86\n"
    "s_nop 0\n"
    "global_load_lds_dwordx4 v201, s[48:49] offset:0 sc1\n"
    "global_load_lds_dwordx4 v201, s[48:49] offset:1024 sc1\n"
    "global_load_lds_dwordx4 v201, s[48:49] offset:2048 sc1\n"
    "global_load_lds_dwordx4 v201, s[48:49] offset:3072 sc1\n"
    "s_add_u32 s48, s48, 0x4000\n"
    "s_addc_u32 s49, s49, 0\n"
    "s_add_u32 m0, s86, 0x4000\n"
    "s_nop 0\n"
    "global_load_lds_dwordx4 v201, s[48:49] offset:0 sc1\n"
    "global_load_lds_dwordx4 v201, s[48:49] offset:1024 sc1\n"
    "global_load_lds_dwordx4 v201, s[48:49] offset:2048 sc1\n"
    "global_load_lds_dwordx4 v201, s[48:49] offset:3072 sc1\n"
    "s_add_u32 s40, s40, 0x8000\n"
    "s_addc_u32 s41, s41, 0\n"
    "s_add_u32 m0, s35, 0x3000\n"
    "s_nop 0\n"
    "global_load_lds_dwordx4 v80, s[52:53] offset:0 nt\n"
    "global_load_lds_dwordx4 v81, s[52:53] offset:1024 nt\n"
    "global_load_lds_dwordx4 v82, s[52:53] offset:2048 nt\n"
    "global_load_lds_dwordx4 v83, s[52:53] offset:3072 nt\n"
    "s_waitcnt vmcnt(16)\n"
    "ds_read_b128 a[16:19], v84 offset:4096\n"
    "ds_read_b128 a[20:23], v85 offset:4096\n"
    "ds_read_b128 a[24:27], v86 offset:4096\n"
    "ds_read_b128 a[28:31], v87 offset:4096\n"
    "s_waitcnt lgkmcnt(3)\n"
    "v_mfma_f32_16x16x32_bf16 v[180:183], v[64:67], a[16:19], v[88:91]\n"
    "s_waitcnt lgkmcnt(2)\n"
    "v_mfma_f32_16x16x32_bf16 v[180:183], v[68:71], a[20:23], v[180:183]\n"
    "s_waitcnt lgkmcnt(1)\n"
    "v_mfma_f32_16x16x32_bf16 v[180:183], v[72:75], a[24:27], v[180:183]\n"
    "s_waitcnt lgkmcnt(0)\n"
    "v_mfma_f32_16x16x32_bf16 v[180:183], v[76:79], a[28:31], v[180:183]\n"
    "s_waitcnt vmcnt(8)\n"
    "s_waitcnt lgkmcnt(0)\n"
    "s_barrier\n"
    "ds_read_b128 a[64:67], v201 offset:0\n"
    "ds_read_b128 a[68:71], v201 offset:1024\n"
    "ds_read_b128 a[72:75], v201 offset:2048\n"
    "ds_read_b128 a[76:79], v201 offset:3072\n"
    "ds_read_b128 a[92:95], v201 offset:7168\n"
    "ds_read_b128 a[88:91], v201 offset:6144\n"
    "ds_read_b128 a[84:87], v201 offset:5120\n"
    "ds_read_b128 a[80:83], v201 offset:4096\n"
    "s_waitcnt lgkmcnt(7)\n"
    "v_mfma_f32_16x16x32_bf16 v[128:131], a[64:67], a[0:3], v[176:179]\n"
    "s_waitcnt lgkmcnt(6)\n"
    "ds_read_b128 a[96:99], v201 offset:8192\n"
    "v_mfma_f32_16x16x32_bf16 v[128:131], a[68:71], a[4:7], v[128:131]\n"
    "s_waitcnt lgkmcnt(6)\n"
    "ds_read_b128 a[100:103], v201 offset:9216\n"
    "v_mfma_f32_16x16x32_bf16 v[128:131], a[72:75], a[8:11], v[128:131]\n"
    "s_waitcnt lgkmcnt(6)\n"
    "ds_read_b128 a[104:107], v201 offset:10240\n"
    "v_mfma_f32_16x16x32_bf16 v[128:131], a[76:79], a[12:15], v[128:131]\n"
    "s_waitcnt lgkmcnt(6)\n"
    "ds_read_b128 a[108:111], v201 offset:11264\n"
    "v_mfma_f32_16x16x32_bf16 v[132:135], a[92:95], a[12:15], v[176:179]\n"
    "s_waitcnt lgkmcnt(6)\n"
    "ds_read_b128 a[124:127], v201 offset:15360\n"
    "v_mfma_f32_16x16x32_bf16 v[132:135], a[88:91], a[8:11], v[132:135]\n"
    "s_waitcnt lgkmcnt(6)\n"
    "ds_read_b128 a[120:123], v201 offset:14336\n"
    "v_mfma_f32_16x16x32_bf16 v[132:135], a[84:87], a[4:7], v[132:135]\n"
    "s_waitcnt lgkmcnt(6)\n"
    "ds_read_b128 a[116:119], v201 offset:13312\n"
    "v_mfma_f32_16x16x32_bf16 v[132:135], a[80:83], a[0:3], v[132:135]\n"
    "s_waitcnt vmcnt(4)\n"
    "ds_read_b128 a[112:115], v201 offset:12288\n"
    "s_waitcnt lgkmcnt(7)\n"
    "v_mfma_f32_16x16x32_bf16 v[136:139], a[96:99], a[0:3], v[176:179]\n"
    "s_barrier\n"
    "s_waitcnt lgkmcnt(6)\n"
    "v_mfma_f32_16x16x32_bf16 v[136:139], a[100:103], a[4:7], v[136:139]\n"
    "s_add_u32 m0, s6, 0x8000\n"
    "ds_read_b128 a[128:131], v201 offset:16384\n"
    "s_waitcnt lgkmcnt(6)\n"
    "v_mfma_f32_16x16x32_bf16 v[136:139], a[104:107], a[8:11], v[136:139]\n"
    "global_load_lds_dwordx4 v201, s[40:41] offset:0 sc1\n"
    "s_waitcnt lgkmcnt(5)\n"
    "v_mfma_f32_16x16x32_bf16 v[136:139], a[108:111], a[12:15], v[136:139]\n"
    "global_load_lds_dwordx4 v201, s[40:41] offset:1024 sc1\n"
    "v_exp_f32 v128, v128\n"
    "s_waitcnt lgkmcnt(4)\n"
    "v_mfma_f32_16x16x32_bf16 v[140:143], a[124:127], a[12:15], v[176:179]\n"
    "s_waitcnt lgkmcnt(3)\n"
    "v_exp_f32 v129, v129\n"
    "v_mfma_f32_16x16x32_bf16 v[140:143], a[120:123], a[8:11], v[140:143]\n"
    "s_waitcnt lgkmcnt(2)\n"
    "v_exp_f32 v130, v130\n"
    "v_mfma_f32_16x16x32_bf16 v[140:143], a[116:119], a[4:7], v[140:143]\n"
    "s_waitcnt lgkmcnt(1)\n"
    "v_exp_f32 v131, v131\n"
    "v_mfma_f32_16x16x32_bf16 v[140:143], a[112:115], a[0:3], v[140:143]\n"
    "v_exp_f32 v132, v132\n"
    "v_mfma_f32_16x16x32_bf16 v[144:147], a[64:67], a[16:19], v[180:183]\n"
    "v_exp_f32 v133, v133\n"
    "v_mfma_f32_16x16x32_bf16 v[144:147], a[68:71], a[20:23], v[144:147]\n"
    "v_exp_f32 v134, v134\n"
    "v_mfma_f32_16x16x32_bf16 v[144:147], a[72:75], a[24:27], v[144:147]\n"
    "global_load_lds_dwordx4 v201, s[40:41] offset:2048 sc1\n"
    "v_cvt_pk_bf16_f32 v160, v128, v129\n"
    "v_cvt_pk_bf16_f32 v161, v130, v131\n"
    "v_mfma_f32_16x16x32_bf16 v[144:147], a[76:79], a[28:31], v[144:147]\n"
    "global_load_lds_dwordx4 v201, s[40:41] offset:3072 sc1\n"
    "v_cvt_pk_bf16_f32 v162, v132, v133\n"
    "v_exp_f32 v135, v135\n"
    "v_mfma_f32_16x16x32_bf16 v[148:151], a[92:95], a[28:31], v[180:183]\n"
    "s_add_u32 m0, s6, 0x9000\n"
    "v_exp_f32 v136, v136\n"
    "v_mfma_f32_16x16x32_bf16 v[148:151], a[88:91], a[24:27], v[148:151]\n"
    "v_exp_f32 v137, v137\n"
    "v_mfma_f32_16x16x32_bf16 v[148:151], a[84:87], a[20:23], v[148:151]\n"
    "v_exp_f32 v138, v138\n"
    "v_mfma_f32_16x16x32_bf16 v[148:151], a[80:83], a[16:19], v[148:151]\n"
    "v_exp_f32 v139, v139\n"
    "v_mfma_f32_16x16x32_bf16 v[152:155], a[96:99], a[16:19], v[180:183]\n"
    "v_exp_f32 v140, v140\n"
    "v_mfma_f32_16x16x32_bf16 v[152:155], a[100:103], a[20:23], v[152:155]\n"
    "v_exp_f32 v141, v141\n"
    "v_mfma_f32_16x16x32_bf16 v[152:155], a[104:107], a[24:27], v[152:155]\n"
    "global_load_lds_dwordx4 v202, s[40:41] offset:0 sc1\n"
    "ds_read_b128 a[132:135], v201 offset:24576\n"
    "v_cvt_pk_bf16_f32 v163, v134, v135\n"
    "v_mfma_f32_16x16x32_bf16 v[152:155], a[108:111], a[28:31], v[152:155]\n"
    "global_load_lds_dwordx4 v202, s[40:41] offset:1024 sc1\n"
    "ds_read_b128 a[136:139], v201 offset:17408\n"
    "v_cvt_pk_bf16_f32 v164, v136, v137\n"
    "v_cvt_pk_bf16_f32 v165, v138, v139\n"
    "v_mfma_f32_16x16x32_bf16 v[156:159], a[124:127], a[28:31], v[180:183]\n"
    "ds_read_b128 a[140:143], v201 offset:25600\n"
    "v_cvt_pk_bf16_f32 v166, v140, v141\n"
    "v_exp_f32 v142, v142\n"
    "v_mfma_f32_16x16x32_bf16 v[156:159], a[120:123], a[24:27], v[156:159]\n"
    "ds_read_b128 a[144:147], v201 offset:18432\n"
    "v_exp_f32 v143, v143\n"
    "v_mfma_f32_16x16x32_bf16 v[156:159], a[116:119], a[20:23], v[156:159]\n"
    "ds_read_b128 a[148:151], v201 offset:26624\n"
    "v_cvt_pk_bf16_f32 v167, v142, v143\n"
    "v_mfma_f32_16x16x32_bf16 v[156:159], a[112:115], a[16:19], v[156:159]\n"
    "s_waitcnt lgkmcnt(5)\n"
    "ds_read_b128 a[152:155], v201 offset:19456\n"
    "v_mfma_f32_16x16x32_bf16 v[0:3], v[160:163], a[128:131], 0\n"
    "s_waitcnt lgkmcnt(5)\n"
    "ds_read_b128 a[156:159], v201 offset:27648\n"
    "v_mfma_f32_16x16x32_bf16 v[0:3], v[164:167], a[132:135], v[0:3]\n"
    "s_waitcnt lgkmcnt(5)\n"
    "ds_read_b128 a[160:163], v201 offset:20480\n"
    "v_mfma_f32_16x16x32_bf16 v[4:7], v[160:163], a[136:139], 0\n"
    "global_load_lds_dwordx4 v202, s[40:41] offset:2048 sc1\n"
    "ds_read_b128 a[164:167], v201 offset:28672\n"
    "s_waitcnt lgkmcnt(6)\n"
    "v_mfma_f32_16x16x32_bf16 v[4:7], v[164:167], a[140:143], v[4:7]\n"
    "global_load_lds_dwordx4 v202, s[40:41] offset:3072 sc1\n"
    "ds_read_b128 a[168:171], v201 offset:21504\n"
    "s_waitcnt lgkmcnt(6)\n"
    "v_mfma_f32_16x16x32_bf16 v[8:11], v[160:163], a[144:147], 0\n"
    "s_add_u32 s40, s40, 0x8000\n"
    "ds_read_b128 a[172:175], v201 offset:29696\n"
    "ds_read_b128 a[176:179], v201 offset:22528\n"
    "ds_read_b128 a[180:183], v201 offset:30720\n"
    "ds_read_b128 a[184:187], v201 offset:23552\n"
    "ds_read_b128 a[188:191], v201 offset:31744\n"
    "ds_read_b128 a[32:35], v84 offset:8192\n"
    "s_waitcnt lgkmcnt(11)\n"
    "v_mfma_f32_16x16x32_bf16 v[8:11], v[164:167], a[148:151], v[8:11]\n"
    "s_addc_u32 s41, s41, 0\n"
    "ds_read_b128 a[36:39], v85 offset:8192\n"
    "s_waitcnt lgkmcnt(11)\n"
    "v_mfma_f32_16x16x32_bf16 v[12:15], v[160:163], a[152:155], 0\n"
    "s_add_u32 m0, s6, 0x10000\n"
    "ds_read_b128 a[40:43], v86 offset:8192\n"
    "s_waitcnt lgkmcnt(11)\n"
    "v_mfma_f32_16x16x32_bf16 v[12:15], v[164:167], a[156:159], v[12:15]\n"
    "s_waitcnt lgkmcnt(10)\n"
    "ds_read_b128 a[44:47], v87 offset:8192\n"
    "v_mfma_f32_16x16x32_bf16 v[16:19], v[160:163], a[160:163], 0\n"
    "s_waitcnt lgkmcnt(10)\n"
    "v_exp_f32 v144, v144\n"
    "v_mfma_f32_16x16x32_bf16 v[16:19], v[164:167], a[164:167], v[16:19]\n"
    "s_waitcnt lgkmcnt(9)\n"
    "v_exp_f32 v145, v145\n"
    "v_mfma_f32_16x16x32_bf16 v[20:23], v[160:163], a[168:171], 0\n"
    "global_load_lds_dwordx4 v201, s[40:41] offset:0 sc1\n"
    "s_waitcnt lgkmcnt(8)\n"
    "v_mfma_f32_16x16x32_bf16 v[20:23], v[164:167], a[172:175], v[20:23]\n"
    "global_load_lds_dwordx4 v201, s[40:41] offset:1024 sc1\n"
    "v_cvt_pk_bf16_f32 v168, v144, v145\n"
    "s_waitcnt lgkmcnt(7)\n"
    "v_mfma_f32_16x16x32_bf16 v[24:27], v[160:163], a[176:179], 0\n"
    "s_waitcnt lgkmcnt(6)\n"
    "v_exp_f32 v146, v146\n"
    "v_mfma_f32_16x16x32_bf16 v[24:27], v[164:167], a[180:183], v[24:27]\n"
    "s_waitcnt lgkmcnt(3)\n"
    "v_exp_f32 v147, v147\n"
    "v_mfma_f32_16x16x32_bf16 v[28:31], v[160:163], a[184:187], 0\n"
    "v_exp_f32 v148, v148\n"
    "v_mfma_f32_16x16x32_bf16 v[28:31], v[164:167], a[188:191], v[28:31]\n"
    "200: s_cbranch_execz 201f\n"
    "v_exp_f32 v149, v149\n"
    "v_smfmac_f32_16x16x64_bf16 a[196:199], a[192:195], v[160:167], v240\n"
    "v_cvt_pk_bf16_f32 v169, v146, v147\n"
    "v_mfma_f32_16x16x32_bf16 v[184:187], v[64:67], a[32:35], v[88:91]\n"
    "s_waitcnt lgkmcnt(0)\n"
    "v_exp_f32 v150, v150\n"
    "v_mfma_f32_16x16x32_bf16 v[184:187], v[68:71], a[36:39], v[184:187]\n"
    "global_load_lds_dwordx4 v201, s[40:41] offset:2048 sc1\n"
    "v_mfma_f32_16x16x32_bf16 v[184:187], v[72:75], a[40:43], v[184:187]\n"
    "global_load_lds_dwordx4 v201, s[40:41] offset:3072 sc1\n"
    "v_cvt_pk_bf16_f32 v170, v148, v149\n"
    "v_mfma_f32_16x16x32_bf16 v[184:187], v[76:79], a[44:47], v[184:187]\n"
    "s_add_u32 m0, s6, 0x11000\n"
    "v_exp_f32 v151, v151\n"
    "v_mfma_f32_16x16x32_bf16 v[128:131], a[64:67], a[32:35], v[184:187]\n"
    "v_exp_f32 v152, v152\n"
    "v_mfma_f32_16x16x32_bf16 v[128:131], a[68:71], a[36:39], v[128:131]\n"
    "v_exp_f32 v153, v153\n"
    "v_mfma_f32_16x16x32_bf16 v[128:131], a[72:75], a[40:43], v[128:131]\n"
    "v_exp_f32 v154, v154\n"
    "v_mfma_f32_16x16x32_bf16 v[128:131], a[76:79], a[44:47], v[128:131]\n"
    "v_exp_f32 v155, v155\n"
    "v_mfma_f32_16x16x32_bf16 v[132:135], a[92:95], a[44:47], v[184:187]\n"
    "v_exp_f32 v156, v156\n"
    "v_mfma_f32_16x16x32_bf16 v[132:135], a[88:91], a[40:43], v[132:135]\n"
    "global_load_lds_dwordx4 v202, s[40:41] offset:0 sc1\n"
    "v_mfma_f32_16x16x32_bf16 v[132:135], a[84:87], a[36:39], v[132:135]\n"
    "global_load_lds_dwordx4 v202, s[40:41] offset:1024 sc1\n"
    "v_cvt_pk_bf16_f32 v171, v150, v151\n"
    "v_mfma_f32_16x16x32_bf16 v[132:135], a[80:83], a[32:35], v[132:135]\n"
    "v_exp_f32 v157, v157\n"
    "v_mfma_f32_16x16x32_bf16 v[136:139], a[96:99], a[32:35], v[184:187]\n"
    "v_exp_f32 v158, v158\n"
    "v_mfma_f32_16x16x32_bf16 v[136:139], a[100:103], a[36:39], v[136:139]\n"
    "v_exp_f32 v159, v159\n"
    "v_mfma_f32_16x16x32_bf16 v[136:139], a[104:107], a[40:43], v[136:139]\n"
    "v_exp_f32 v128, v128\n"
    "v_mfma_f32_16x16x32_bf16 v[136:139], a[108:111], a[44:47], v[136:139]\n"
    "v_cvt_pk_bf16_f32 v172, v152, v153\n"
    "v_cvt_pk_bf16_f32 v173, v154, v155\n"
    "v_mfma_f32_16x16x32_bf16 v[140:143], a[124:127], a[44:47], v[184:187]\n"
    "v_exp_f32 v129, v129\n"
    "v_mfma_f32_16x16x32_bf16 v[140:143], a[120:123], a[40:43], v[140:143]\n"
    "global_load_lds_dwordx4 v202, s[40:41] offset:2048 sc1\n"
    "v_mfma_f32_16x16x32_bf16 v[140:143], a[116:119], a[36:39], v[140:143]\n"
    "global_load_lds_dwordx4 v202, s[40:41] offset:3072 sc1\n"
    "v_cvt_pk_bf16_f32 v174, v156, v157\n"
    "v_cvt_pk_bf16_f32 v175, v158, v159\n"
    "v_mfma_f32_16x16x32_bf16 v[140:143], a[112:115], a[32:35], v[140:143]\n"
    "s_add_u32 s40, s40, 0x8000\n"
    "v_exp_f32 v130, v130\n"
    "v_mfma_f32_16x16x32_bf16 v[32:35], v[168:171], a[128:131], 0\n"
    "s_addc_u32 s41, s41, 0\n"
    "v_exp_f32 v131, v131\n"
    "v_mfma_f32_16x16x32_bf16 v[32:35], v[172:175], a[132:135], v[32:35]\n"
    "v_cvt_pk_bf16_f32 v160, v128, v129\n"
    "v_cvt_pk_bf16_f32 v161, v130, v131\n"
    "v_mfma_f32_16x16x32_bf16 v[36:39], v[168:171], a[136:139], 0\n"
    "v_exp_f32 v132, v132\n"
    "v_mfma_f32_16x16x32_bf16 v[36:39], v[172:175], a[140:143], v[36:39]\n"
    "v_exp_f32 v133, v133\n"
    "v_mfma_f32_16x16x32_bf16 v[40:43], v[168:171], a[144:147], 0\n"
    "s_waitcnt vmcnt(16)\n"
    "ds_read_b128 a[48:51], v84 offset:12288\n"
    "v_cvt_pk_bf16_f32 v162, v132, v133\n"
    "v_mfma_f32_16x16x32_bf16 v[40:43], v[172:175], a[148:151], v[40:43]\n"
    "s_add_u32 m0, s6, 0x0\n"
    "ds_read_b128 a[52:55], v85 offset:12288\n"
    "v_mfma_f32_16x16x32_bf16 v[44:47], v[168:171], a[152:155], 0\n"
    "ds_read_b128 a[56:59], v86 offset:12288\n"
    "v_mfma_f32_16x16x32_bf16 v[44:47], v[172:175], a[156:159], v[44:47]\n"
    "s_waitcnt vmcnt(8)\n"
    "ds_read_b128 a[60:63], v87 offset:12288\n"
    "v_mfma_f32_16x16x32_bf16 v[48:51], v[168:171], a[160:163], 0\n"
    "s_barrier\n"
    "v_mfma_f32_16x16x32_bf16 v[48:51], v[172:175], a[164:167], v[48:51]\n"
    "global_load_lds_dwordx4 v201, s[40:41] offset:0 sc1\n"
    "v_mfma_f32_16x16x32_bf16 v[52:55], v[168:171], a[168:171], 0\n"
    "global_load_lds_dwordx4 v201, s[40:41] offset:1024 sc1\n"
    "v_mfma_f32_16x16x32_bf16 v[52:55], v[172:175], a[172:175], v[52:55]\n"
    "v_exp_f32 v134, v134\n"
    "v_mfma_f32_16x16x32_bf16 v[56:59], v[168:171], a[176:179], 0\n"
    "v_exp_f32 v135, v135\n"
    "v_mfma_f32_16x16x32_bf16 v[56:59], v[172:175], a[180:183], v[56:59]\n"
    "v_exp_f32 v136, v136\n"
    "v_mfma_f32_16x16x32_bf16 v[60:63], v[168:171], a[184:187], 0\n"
    "v_exp_f32 v137, v137\n"
    "v_mfma_f32_16x16x32_bf16 v[60:63], v[172:175], a[188:191], v[60:63]\n"
    "v_exp_f32 v138, v138\n"
    "v_smfmac_f32_16x16x64_bf16 a[200:203], a[192:195], v[168:175], v240\n"
    "s_waitcnt lgkmcnt(0)\n"
    "v_cvt_pk_bf16_f32 v163, v134, v135\n"
    "v_mfma_f32_16x16x32_bf16 v[188:191], v[64:67], a[48:51], v[88:91]\n"
    "global_load_lds_dwordx4 v201, s[40:41] offset:2048 sc1\n"
    "v_mfma_f32_16x16x32_bf16 v[188:191], v[68:71], a[52:55], v[188:191]\n"
    "global_load_lds_dwordx4 v201, s[40:41] offset:3072 sc1\n"
    "v_cvt_pk_bf16_f32 v164, v136, v137\n"
    "v_mfma_f32_16x16x32_bf16 v[188:191], v[72:75], a[56:59], v[188:191]\n"
    "s_add_u32 m0, s6, 0x1000\n"
    "v_exp_f32 v139, v139\n"
    "v_mfma_f32_16x16x32_bf16 v[188:191], v[76:79], a[60:63], v[188:191]\n"
    "v_exp_f32 v140, v140\n"
    "v_mfma_f32_16x16x32_bf16 v[144:147], a[64:67], a[48:51], v[188:191]\n"
    "v_exp_f32 v141, v141\n"
    "v_mfma_f32_16x16x32_bf16 v[144:147], a[68:71], a[52:55], v[144:147]\n"
    "v_exp_f32 v142, v142\n"
    "v_mfma_f32_16x16x32_bf16 v[144:147], a[72:75], a[56:59], v[144:147]\n"
    "ds_read_b128 a[64:67], v201 offset:32768\n"
    "v_cvt_pk_bf16_f32 v165, v138, v139\n"
    "v_mfma_f32_16x16x32_bf16 v[144:147], a[76:79], a[60:63], v[144:147]\n"
    "v_exp_f32 v143, v143\n"
    "v_mfma_f32_16x16x32_bf16 v[148:151], a[92:95], a[60:63], v[188:191]\n"
    "global_load_lds_dwordx4 v202, s[40:41] offset:0 sc1\n"
    "v_mfma_f32_16x16x32_bf16 v[148:151], a[88:91], a[56:59], v[148:151]\n"
    "global_load_lds_dwordx4 v202, s[40:41] offset:1024 sc1\n"
    "v_cvt_pk_bf16_f32 v166, v140, v141\n"
    "v_mfma_f32_16x16x32_bf16 v[148:151], a[84:87], a[52:55], v[148:151]\n"
    "ds_read_b128 a[68:71], v201 offset:33792\n"
    "v_cvt_pk_bf16_f32 v167, v142, v143\n"
    "v_mfma_f32_16x16x32_bf16 v[148:151], a[80:83], a[48:51], v[148:151]\n"
    "ds_read_b128 a[72:75], v201 offset:34816\n"
    "v_mfma_f32_16x16x32_bf16 v[152:155], a[96:99], a[48:51], v[188:191]\n"
    "ds_read_b128 a[76:79], v201 offset:35840\n"
    "v_mfma_f32_16x16x32_bf16 v[152:155], a[100:103], a[52:55], v[152:155]\n"
    "v_mfma_f32_16x16x32_bf16 v[152:155], a[104:107], a[56:59], v[152:155]\n"
    "v_exp_f32 v144, v144\n"
    "v_mfma_f32_16x16x32_bf16 v[152:155], a[108:111], a[60:63], v[152:155]\n"
    "ds_read_b128 a[92:95], v201 offset:39936\n"
    "v_mfma_f32_16x16x32_bf16 v[156:159], a[124:127], a[60:63], v[188:191]\n"
    "global_load_lds_dwordx4 v202, s[40:41] offset:2048 sc1\n"
    "v_mfma_f32_16x16x32_bf16 v[156:159], a[120:123], a[56:59], v[156:159]\n"
    "global_load_lds_dwordx4 v202, s[40:41] offset:3072 sc1\n"
    "v_mfma_f32_16x16x32_bf16 v[156:159], a[116:119], a[52:55], v[156:159]\n"
    "s_add_u32 s40, s40, 0x8000\n"
    "ds_read_b128 a[88:91], v201 offset:38912\n"
    "v_mfma_f32_16x16x32_bf16 v[156:159], a[112:115], a[48:51], v[156:159]\n"
    "s_addc_u32 s41, s41, 0\n"
    "v_exp_f32 v145, v145\n"
    "v_mfma_f32_16x16x32_bf16 v[64:67], v[160:163], a[128:131], 0\n"
    "ds_read_b128 a[84:87], v201 offset:37888\n"
    "v_cvt_pk_bf16_f32 v168, v144, v145\n"
    "v_mfma_f32_16x16x32_bf16 v[64:67], v[164:167], a[132:135], v[64:67]\n"
    "v_exp_f32 v146, v146\n"
    "v_mfma_f32_16x16x32_bf16 v[68:71], v[160:163], a[136:139], 0\n"
    "ds_read_b128 a[80:83], v201 offset:36864\n"
    "v_mfma_f32_16x16x32_bf16 v[68:71], v[164:167], a[140:143], v[68:71]\n"
    "v_exp_f32 v147, v147\n"
    "v_mfma_f32_16x16x32_bf16 v[72:75], v[160:163], a[144:147], 0\n"
    "ds_read_b128 a[96:99], v201 offset:40960\n"
    "v_cvt_pk_bf16_f32 v169, v146, v147\n"
    "v_mfma_f32_16x16x32_bf16 v[72:75], v[164:167], a[148:151], v[72:75]\n"
    "v_exp_f32 v148, v148\n"
    "v_mfma_f32_16x16x32_bf16 v[76:79], v[160:163], a[152:155], 0\n"
    "ds_read_b128 a[100:103], v201 offset:41984\n"
    "v_mfma_f32_16x16x32_bf16 v[76:79], v[164:167], a[156:159], v[76:79]\n"
    "v_exp_f32 v149, v149\n"
    "v_mfma_f32_16x16x32_bf16 v[80:83], v[160:163], a[160:163], 0\n"
    "ds_read_b128 a[104:107], v201 offset:43008\n"
    "v_cvt_pk_bf16_f32 v170, v148, v149\n"
    "v_mfma_f32_16x16x32_bf16 v[80:83], v[164:167], a[164:167], v[80:83]\n"
    "v_exp_f32 v150, v150\n"
    "v_mfma_f32_16x16x32_bf16 v[84:87], v[160:163], a[168:171], 0\n"
    "ds_read_b128 a[108:111], v201 offset:44032\n"
    "v_mfma_f32_16x16x32_bf16 v[84:87], v[164:167], a[172:175], v[84:87]\n"
    "v_exp_f32 v151, v151\n"
    "v_mfma_f32_16x16x32_bf16 v[88:91], v[160:163], a[176:179], 0\n"
    "ds_read_b128 a[124:127], v201 offset:48128\n"
    "v_cvt_pk_bf16_f32 v171, v150, v151\n"
    "v_mfma_f32_16x16x32_bf16 v[88:91], v[164:167], a[180:183], v[88:91]\n"
    "v_exp_f32 v152, v152\n"
    "v_mfma_f32_16x16x32_bf16 v[92:95], v[160:163], a[184:187], 0\n"
    "v_exp_f32 v153, v153\n"
    "v_mfma_f32_16x16x32_bf16 v[92:95], v[164:167], a[188:191], v[92:95]\n"
    "ds_read_b128 a[120:123], v201 offset:47104\n"
    "v_cvt_pk_bf16_f32 v172, v152, v153\n"
    "v_smfmac_f32_16x16x64_bf16 a[204:207], a[192:195], v[160:167], v240\n"
    "s_waitcnt lgkmcnt(4)\n"
    "ds_read_b128 a[116:119], v201 offset:46080\n"
    "v_mfma_f32_16x16x32_bf16 v[128:131], a[64:67], a[0:3], v[176:179]\n"
    "v_exp_f32 v154, v154\n"
    "v_mfma_f32_16x16x32_bf16 v[128:131], a[68:71], a[4:7], v[128:131]\n"
    "s_add_u32 m0, s35, 0x0\n"
    "v_exp_f32 v155, v155\n"
    "v_mfma_f32_16x16x32_bf16 v[128:131], a[72:75], a[8:11], v[128:131]\n"
    "global_load_lds_dwordx4 v241, s[54:55] offset:0\n"
    "v_mfma_f32_16x16x32_bf16 v[128:131], a[76:79], a[12:15], v[128:131]\n"
    "global_load_lds_dwordx4 v241, s[58:59] offset:1024\n"
    "v_cvt_pk_bf16_f32 v173, v154, v155\n"
    "v_xor_b32 v241, 0x6000, v241\n"
    "v_mfma_f32_16x16x32_bf16 v[132:135], a[92:95], a[12:15], v[176:179]\n"
    "v_exp_f32 v156, v156\n"
    "v_mfma_f32_16x16x32_bf16 v[132:135], a[88:91], a[8:11], v[132:135]\n"
    "ds_read_b128 a[112:115], v201 offset:45056\n"
    "v_mfma_f32_16x16x32_bf16 v[132:135], a[84:87], a[4:7], v[132:135]\n"
    "v_exp_f32 v157, v157\n"
    "v_mfma_f32_16x16x32_bf16 v[132:135], a[80:83], a[0:3], v[132:135]\n"
    "v_exp_f32 v158, v158\n"
    "v_mfma_f32_16x16x32_bf16 v[136:139], a[96:99], a[0:3], v[176:179]\n"
    "v_exp_f32 v159, v159\n"
    "v_mfma_f32_16x16x32_bf16 v[136:139], a[100:103], a[4:7], v[136:139]\n"
    "s_waitcnt lgkmcnt(1)\n"
    "v_cvt_pk_bf16_f32 v174, v156, v157\n"
    "v_cvt_pk_bf16_f32 v175, v158, v159\n"
    "v_mfma_f32_16x16x32_bf16 v[136:139], a[104:107], a[8:11], v[136:139]\n"
    "v_exp_f32 v128, v128\n"
    "v_mfma_f32_16x16x32_bf16 v[136:139], a[108:111], a[12:15], v[136:139]\n"
    "v_exp_f32 v129, v129\n"
    "v_mfma_f32_16x16x32_bf16 v[140:143], a[124:127], a[12:15], v[176:179]\n"
    "v_cvt_pk_bf16_f32 v160, v128, v129\n"
    "v_mfma_f32_16x16x32_bf16 v[140:143], a[120:123], a[8:11], v[140:143]\n"
    "v_exp_f32 v130, v130\n"
    "v_mfma_f32_16x16x32_bf16 v[140:143], a[116:119], a[4:7], v[140:143]\n"
    "s_waitcnt lgkmcnt(0)\n"
    "v_exp_f32 v131, v131\n"
    "v_mfma_f32_16x16x32_bf16 v[140:143], a[112:115], a[0:3], v[140:143]\n"
    "v_cvt_pk_bf16_f32 v161, v130, v131\n"
    "v_mfma_f32_16x16x32_bf16 v[96:99], v[168:171], a[128:131], 0\n"
    "v_exp_f32 v132, v132\n"
    "v_mfma_f32_16x16x32_bf16 v[96:99], v[172:175], a[132:135], v[96:99]\n"
    "ds_read_b128 a[128:131], v201 offset:49152\n"
    "v_mfma_f32_16x16x32_bf16 v[100:103], v[168:171], a[136:139], 0\n"
    "v_exp_f32 v133, v133\n"
    "v_mfma_f32_16x16x32_bf16 v[100:103], v[172:175], a[140:143], v[100:103]\n"
    "ds_read_b128 a[132:135], v201 offset:57344\n"
    "v_cvt_pk_bf16_f32 v162, v132, v133\n"
    "v_mfma_f32_16x16x32_bf16 v[104:107], v[168:171], a[144:147], 0\n"
    "v_exp_f32 v134, v134\n"
    "v_mfma_f32_16x16x32_bf16 v[104:107], v[172:175], a[148:151], v[104:107]\n"
    "ds_read_b128 a[136:139], v201 offset:50176\n"
    "v_mfma_f32_16x16x32_bf16 v[108:111], v[168:171], a[152:155], 0\n"
    "v_exp_f32 v135, v135\n"
    "v_mfma_f32_16x16x32_bf16 v[108:111], v[172:175], a[156:159], v[108:111]\n"
    "v_cvt_pk_bf16_f32 v163, v134, v135\n"
    "v_mfma_f32_16x16x32_bf16 v[112:115], v[168:171], a[160:163], 0\n"
    "ds_read_b128 a[140:143], v201 offset:58368\n"
    "v_mfma_f32_16x16x32_bf16 v[112:115], v[172:175], a[164:167], v[112:115]\n"
    "v_exp_f32 v136, v136\n"
    "v_mfma_f32_16x16x32_bf16 v[116:119], v[168:171], a[168:171], 0\n"
    "ds_read_b128 a[144:147], v201 offset:51200\n"
    "v_mfma_f32_16x16x32_bf16 v[116:119], v[172:175], a[172:175], v[116:119]\n"
    "v_exp_f32 v137, v137\n"
    "v_mfma_f32_16x16x32_bf16 v[120:123], v[168:171], a[176:179], 0\n"
    "ds_read_b128 a[148:151], v201 offset:59392\n"
    "v_cvt_pk_bf16_f32 v164, v136, v137\n"
    "v_mfma_f32_16x16x32_bf16 v[120:123], v[172:175], a[180:183], v[120:123]\n"
    "v_exp_f32 v138, v138\n"
    "v_mfma_f32_16x16x32_bf16 v[124:127], v[168:171], a[184:187], 0\n"
    "ds_read_b128 a[152:155], v201 offset:52224\n"
    "v_mfma_f32_16x16x32_bf16 v[124:127], v[172:175], a[188:191], v[124:127]\n"
    "v_exp_f32 v139, v139\n"
    "v_smfmac_f32_16x16x64_bf16 a[208:211], a[192:195], v[168:175], v240\n"
    "ds_read_b128 a[156:159], v201 offset:60416\n"
    "v_mfma_f32_16x16x32_bf16 v[144:147], a[64:67], a[16:19], v[180:183]\n"
    "v_cvt_pk_bf16_f32 v165, v138, v139\n"
    "v_mfma_f32_16x16x32_bf16 v[144:147], a[68:71], a[20:23], v[144:147]\n"
    "v_exp_f32 v140, v140\n"
    "v_mfma_f32_16x16x32_bf16 v[144:147], a[72:75], a[24:27], v[144:147]\n"
    "ds_read_b128 a[160:163], v201 offset:53248\n"
    "v_mfma_f32_16x16x32_bf16 v[144:147], a[76:79], a[28:31], v[144:147]\n"
    "v_exp_f32 v141, v141\n"
    "v_mfma_f32_16x16x32_bf16 v[148:151], a[92:95], a[28:31], v[180:183]\n"
    "ds_read_b128 a[164:167], v201 offset:61440\n"
    "v_cvt_pk_bf16_f32 v166, v140, v141\n"
    "v_mfma_f32_16x16x32_bf16 v[148:151], a[88:91], a[24:27], v[148:151]\n"
    "v_exp_f32 v142, v142\n"
    "v_mfma_f32_16x16x32_bf16 v[148:151], a[84:87], a[20:23], v[148:151]\n"
    "s_add_u32 m0, s35, 0x0\n"
    "ds_read_b128 a[168:171], v201 offset:54272\n"
    "v_mfma_f32_16x16x32_bf16 v[148:151], a[80:83], a[16:19], v[148:151]\n"
    "global_load_lds_dwordx4 v242, s[56:57] offset:2048\n"
    "v_mfma_f32_16x16x32_bf16 v[152:155], a[96:99], a[16:19], v[180:183]\n"
    "global_load_lds_dwordx4 v242, s[84:85] offset:3072\n"
    "v_xor_b32 v242, 0x6000, v242\n"
    "v_mfma_f32_16x16x32_bf16 v[152:155], a[100:103], a[20:23], v[152:155]\n"
    "ds_read_b128 a[172:175], v201 offset:62464\n"
    "v_mfma_f32_16x16x32_bf16 v[152:155], a[104:107], a[24:27], v[152:155]\n"
    "v_exp_f32 v143, v143\n"
    "v_mfma_f32_16x16x32_bf16 v[152:155], a[108:111], a[28:31], v[152:155]\n"
    "ds_read_b128 a[176:179], v201 offset:55296\n"
    "v_cvt_pk_bf16_f32 v167, v142, v143\n"
    "v_mfma_f32_16x16x32_bf16 v[156:159], a[124:127], a[28:31], v[180:183]\n"
    "v_exp_f32 v144, v144\n"
    "v_mfma_f32_16x16x32_bf16 v[156:159], a[120:123], a[24:27], v[156:159]\n"
    "v_exp_f32 v145, v145\n"
    "v_mfma_f32_16x16x32_bf16 v[156:159], a[116:119], a[20:23], v[156:159]\n"
    "ds_read_b128 a[180:183], v201 offset:63488\n"
    "v_cvt_pk_bf16_f32 v168, v144, v145\n"
    "v_mfma_f32_16x16x32_bf16 v[156:159], a[112:115], a[16:19], v[156:159]\n"
    "s_waitcnt lgkmcnt(3)\n"
    "v_exp_f32 v146, v146\n"
    "v_mfma_f32_16x16x32_bf16 v[0:3], v[160:163], a[128:131], v[0:3]\n"
    "ds_read_b128 a[184:187], v201 offset:56320\n"
    "v_mfma_f32_16x16x32_bf16 v[0:3], v[164:167], a[132:135], v[0:3]\n"
    "v_exp_f32 v147, v147\n"
    "v_mfma_f32_16x16x32_bf16 v[4:7], v[160:163], a[136:139], v[4:7]\n"
    "ds_read_b128 a[188:191], v201 offset:64512\n"
    "v_cvt_pk_bf16_f32 v169, v146, v147\n"
    "v_mfma_f32_16x16x32_bf16 v[4:7], v[164:167], a[140:143], v[4:7]\n"
    "v_exp_f32 v148, v148\n"
    "v_mfma_f32_16x16x32_bf16 v[8:11], v[160:163], a[144:147], v[8:11]\n"
    "v_exp_f32 v149, v149\n"
    "v_mfma_f32_16x16x32_bf16 v[8:11], v[164:167], a[148:151], v[8:11]\n"
    "v_cvt_pk_bf16_f32 v170, v148, v149\n"
    "v_mfma_f32_16x16x32_bf16 v[12:15], v[160:163], a[152:155], v[12:15]\n"
    "v_exp_f32 v150, v150\n"
    "v_mfma_f32_16x16x32_bf16 v[12:15], v[164:167], a[156:159], v[12:15]\n"
    "v_exp_f32 v151, v151\n"
    "v_mfma_f32_16x16x32_bf16 v[16:19], v[160:163], a[160:163], v[16:19]\n"
    "v_cvt_pk_bf16_f32 v171, v150, v151\n"
    "v_mfma_f32_16x16x32_bf16 v[16:19], v[164:167], a[164:167], v[16:19]\n"
    "v_exp_f32 v152, v152\n"
    "v_mfma_f32_16x16x32_bf16 v[20:23], v[160:163], a[168:171], v[20:23]\n"
    "s_waitcnt lgkmcnt(1)\n"
    "v_exp_f32 v153, v153\n"
    "v_mfma_f32_16x16x32_bf16 v[20:23], v[164:167], a[172:175], v[20:23]\n"
    "v_cvt_pk_bf16_f32 v172, v152, v153\n"
    "v_mfma_f32_16x16x32_bf16 v[24:27], v[160:163], a[176:179], v[24:27]\n"
    "v_exp_f32 v154, v154\n"
    "v_mfma_f32_16x16x32_bf16 v[24:27], v[164:167], a[180:183], v[24:27]\n"
    "v_exp_f32 v155, v155\n"
    "v_mfma_f32_16x16x32_bf16 v[28:31], v[160:163], a[184:187], v[28:31]\n"
    "s_waitcnt lgkmcnt(0)\n"
    "v_cvt_pk_bf16_f32 v173, v154, v155\n"
    "v_mfma_f32_16x16x32_bf16 v[28:31], v[164:167], a[188:191], v[28:31]\n"
    "v_exp_f32 v156, v156\n"
    "v_smfmac_f32_16x16x64_bf16 a[196:199], a[192:195], v[160:167], v240\n"
    "s_add_u32 m0, s6, 0x8000\n"
    "v_mfma_f32_16x16x32_bf16 v[128:131], a[64:67], a[32:35], v[184:187]\n"
    "v_exp_f32 v157, v157\n"
    "v_mfma_f32_16x16x32_bf16 v[128:131], a[68:71], a[36:39], v[128:131]\n"
    "s_waitcnt vmcnt(12)\n"
    "v_cvt_pk_bf16_f32 v174, v156, v157\n"
    "v_mfma_f32_16x16x32_bf16 v[128:131], a[72:75], a[40:43], v[128:131]\n"
    "s_barrier\n"
    "v_mfma_f32_16x16x32_bf16 v[128:131], a[76:79], a[44:47], v[128:131]\n"
    "global_load_lds_dwordx4 v201, s[40:41] offset:0 sc1\n"
    "v_mfma_f32_16x16x32_bf16 v[132:135], a[92:95], a[44:47], v[184:187]\n"
    "v_exp_f32 v158, v158\n"
    "v_mfma_f32_16x16x32_bf16 v[132:135], a[88:91], a[40:43], v[132:135]\n"
    "v_exp_f32 v159, v159\n"
    "v_mfma_f32_16x16x32_bf16 v[132:135], a[84:87], a[36:39], v[132:135]\n"
    "v_cvt_pk_bf16_f32 v175, v158, v159\n"
    "v_mfma_f32_16x16x32_bf16 v[132:135], a[80:83], a[32:35], v[132:135]\n"
    "v_mfma_f32_16x16x32_bf16 v[136:139], a[96:99], a[32:35], v[184:187]\n"
    "v_mfma_f32_16x16x32_bf16 v[136:139], a[100:103], a[36:39], v[136:139]\n"
    "v_mfma_f32_16x16x32_bf16 v[136:139], a[104:107], a[40:43], v[136:139]\n"
    "global_load_lds_dwordx4 v201, s[40:41] offset:1024 sc1\n"
    "v_mfma_f32_16x16x32_bf16 v[136:139], a[108:111], a[44:47], v[136:139]\n"
    "v_exp_f32 v128, v128\n"
    "v_mfma_f32_16x16x32_bf16 v[140:143], a[124:127], a[44:47], v[184:187]\n"
    "v_exp_f32 v129, v129\n"
    "v_mfma_f32_16x16x32_bf16 v[140:143], a[120:123], a[40:43], v[140:143]\n"
    "v_cvt_pk_bf16_f32 v160, v128, v129\n"
    "v_mfma_f32_16x16x32_bf16 v[140:143], a[116:119], a[36:39], v[140:143]\n"
    "v_exp_f32 v130, v130\n"
    "v_mfma_f32_16x16x32_bf16 v[140:143], a[112:115], a[32:35], v[140:143]\n"
    "v_exp_f32 v131, v131\n"
    "v_mfma_f32_16x16x32_bf16 v[32:35], v[168:171], a[128:131], v[32:35]\n"
    "v_cvt_pk_bf16_f32 v161, v130, v131\n"
    "v_mfma_f32_16x16x32_bf16 v[32:35], v[172:175], a[132:135], v[32:35]\n"
    "global_load_lds_dwordx4 v201, s[40:41] offset:2048 sc1\n"
    "v_mfma_f32_16x16x32_bf16 v[36:39], v[168:171], a[136:139], v[36:39]\n"
    "global_load_lds_dwordx4 v201, s[40:41] offset:3072 sc1\n"
    "v_mfma_f32_16x16x32_bf16 v[36:39], v[172:175], a[140:143], v[36:39]\n"
    "s_add_u32 m0, s6, 0x9000\n"
    "v_exp_f32 v132, v132\n"
    "v_mfma_f32_16x16x32_bf16 v[40:43], v[168:171], a[144:147], v[40:43]\n"
    "v_exp_f32 v133, v133\n"
    "v_mfma_f32_16x16x32_bf16 v[40:43], v[172:175], a[148:151], v[40:43]\n"
    "v_cvt_pk_bf16_f32 v162, v132, v133\n"
    "v_mfma_f32_16x16x32_bf16 v[44:47], v[168:171], a[152:155], v[44:47]\n"
    "v_exp_f32 v134, v134\n"
    "v_mfma_f32_16x16x32_bf16 v[44:47], v[172:175], a[156:159], v[44:47]\n"
    "v_exp_f32 v135, v135\n"
    "v_mfma_f32_16x16x32_bf16 v[48:51], v[168:171], a[160:163], v[48:51]\n"
    "v_cvt_pk_bf16_f32 v163, v134, v135\n"
    "v_mfma_f32_16x16x32_bf16 v[48:51], v[172:175], a[164:167], v[48:51]\n"
    "global_load_lds_dwordx4 v202, s[40:41] offset:0 sc1\n"
    "v_mfma_f32_16x16x32_bf16 v[52:55], v[168:171], a[168:171], v[52:55]\n"
    "v_exp_f32 v136, v136\n"
    "v_mfma_f32_16x16x32_bf16 v[52:55], v[172:175], a[172:175], v[52:55]\n"
    "v_exp_f32 v137, v137\n"
    "v_mfma_f32_16x16x32_bf16 v[56:59], v[168:171], a[176:179], v[56:59]\n"
    "v_cvt_pk_bf16_f32 v164, v136, v137\n"
    "v_mfma_f32_16x16x32_bf16 v[56:59], v[172:175], a[180:183], v[56:59]\n"
    "v_exp_f32 v138, v138\n"
    "v_mfma_f32_16x16x32_bf16 v[60:63], v[168:171], a[184:187], v[60:63]\n"
    "v_exp_f32 v139, v139\n"
    "v_mfma_f32_16x16x32_bf16 v[60:63], v[172:175], a[188:191], v[60:63]\n"
    "v_cvt_pk_bf16_f32 v165, v138, v139\n"
    "v_smfmac_f32_16x16x64_bf16 a[200:203], a[192:195], v[168:175], v240\n"
    "v_mfma_f32_16x16x32_bf16 v[144:147], a[64:67], a[48:51], v[188:191]\n"
    "global_load_lds_dwordx4 v202, s[40:41] offset:1024 sc1\n"
    "v_mfma_f32_16x16x32_bf16 v[144:147], a[68:71], a[52:55], v[144:147]\n"
    "v_exp_f32 v140, v140\n"
    "v_mfma_f32_16x16x32_bf16 v[144:147], a[72:75], a[56:59], v[144:147]\n"
    "ds_read_b128 a[64:67], v203 offset:0\n"
    "v_mfma_f32_16x16x32_bf16 v[144:147], a[76:79], a[60:63], v[144:147]\n"
    "v_exp_f32 v141, v141\n"
    "v_mfma_f32_16x16x32_bf16 v[148:151], a[92:95], a[60:63], v[188:191]\n"
    "ds_read_b128 a[68:71], v203 offset:1024\n"
    "v_cvt_pk_bf16_f32 v166, v140, v141\n"
    "v_mfma_f32_16x16x32_bf16 v[148:151], a[88:91], a[56:59], v[148:151]\n"
    "v_exp_f32 v142, v142\n"
    "v_mfma_f32_16x16x32_bf16 v[148:151], a[84:87], a[52:55], v[148:151]\n"
    "ds_read_b128 a[72:75], v203 offset:2048\n"
    "v_mfma_f32_16x16x32_bf16 v[148:151], a[80:83], a[48:51], v[148:151]\n"
    "global_load_lds_dwordx4 v202, s[40:41] offset:2048 sc1\n"
    "v_mfma_f32_16x16x32_bf16 v[152:155], a[96:99], a[48:51], v[188:191]\n"
    "v_exp_f32 v143, v143\n"
    "v_mfma_f32_16x16x32_bf16 v[152:155], a[100:103], a[52:55], v[152:155]\n"
    "ds_read_b128 a[76:79], v203 offset:3072\n"
    "v_cvt_pk_bf16_f32 v167, v142, v143\n"
    "v_mfma_f32_16x16x32_bf16 v[152:155], a[104:107], a[56:59], v[152:155]\n"
    "v_exp_f32 v144, v144\n"
    "v_mfma_f32_16x16x32_bf16 v[152:155], a[108:111], a[60:63], v[152:155]\n"
    "ds_read_b128 a[92:95], v203 offset:7168\n"
    "v_mfma_f32_16x16x32_bf16 v[156:159], a[124:127], a[60:63], v[188:191]\n"
    "v_exp_f32 v145, v145\n"
    "v_mfma_f32_16x16x32_bf16 v[156:159], a[120:123], a[56:59], v[156:159]\n"
    "ds_read_b128 a[88:91], v203 offset:6144\n"
    "v_cvt_pk_bf16_f32 v168, v144, v145\n"
    "v_mfma_f32_16x16x32_bf16 v[156:159], a[116:119], a[52:55], v[156:159]\n"
    "global_load_lds_dwordx4 v202, s[40:41] offset:3072 sc1\n"
    "v_mfma_f32_16x16x32_bf16 v[156:159], a[112:115], a[48:51], v[156:159]\n"
    "s_add_u32 s40, s40, 0x8000\n"
    "ds_read_b128 a[84:87], v203 offset:5120\n"
    "v_mfma_f32_16x16x32_bf16 v[64:67], v[160:163], a[128:131], v[64:67]\n"
    "s_addc_u32 s41, s41, 0\n"
    "v_exp_f32 v146, v146\n"
    "v_mfma_f32_16x16x32_bf16 v[64:67], v[164:167], a[132:135], v[64:67]\n"
    "ds_read_b128 a[80:83], v203 offset:4096\n"
    "v_mfma_f32_16x16x32_bf16 v[68:71], v[160:163], a[136:139], v[68:71]\n"
    "v_exp_f32 v147, v147\n"
    "v_mfma_f32_16x16x32_bf16 v[68:71], v[164:167], a[140:143], v[68:71]\n"
    "v_exp_f32 v148, v148\n"
    "v_mfma_f32_16x16x32_bf16 v[72:75], v[160:163], a[144:147], v[72:75]\n"
    "ds_read_b128 a[96:99], v203 offset:8192\n"
    "v_cvt_pk_bf16_f32 v169, v146, v147\n"
    "v_mfma_f32_16x16x32_bf16 v[72:75], v[164:167], a[148:151], v[72:75]\n"
    "v_exp_f32 v149, v149\n"
    "v_mfma_f32_16x16x32_bf16 v[76:79], v[160:163], a[152:155], v[76:79]\n"
    "ds_read_b128 a[100:103], v203 offset:9216\n"
    "v_cvt_pk_bf16_f32 v170, v148, v149\n"
    "v_mfma_f32_16x16x32_bf16 v[76:79], v[164:167], a[156:159], v[76:79]\n"
    "v_exp_f32 v150, v150\n"
    "v_mfma_f32_16x16x32_bf16 v[80:83], v[160:163], a[160:163], v[80:83]\n"
    "ds_read_b128 a[104:107], v203 offset:10240\n"
    "v_mfma_f32_16x16x32_bf16 v[80:83], v[164:167], a[164:167], v[80:83]\n"
    "v_exp_f32 v151, v151\n"
    "v_mfma_f32_16x16x32_bf16 v[84:87], v[160:163], a[168:171], v[84:87]\n"
    "ds_read_b128 a[108:111], v203 offset:11264\n"
    "v_cvt_pk_bf16_f32 v171, v150, v151\n"
    "v_mfma_f32_16x16x32_bf16 v[84:87], v[164:167], a[172:175], v[84:87]\n"
    "s_waitcnt vmcnt(10)\n"
    "ds_read_b128 v[204:207], v249 offset:0\n"
    "v_mfma_f32_16x16x32_bf16 v[88:91], v[160:163], a[176:179], v[88:91]\n"
    "ds_read_b128 v[208:211], v249 offset:16\n"
    "v_mfma_f32_16x16x32_bf16 v[88:91], v[164:167], a[180:183], v[88:91]\n"
    "ds_read_b128 a[124:127], v203 offset:15360\n"
    "s_waitcnt vmcnt(8)\n"
    "s_waitcnt lgkmcnt(6)\n"
    "ds_read2st64_b32 v[212:213], v251 offset0:0 offset1:1\n"
    "v_mfma_f32_16x16x32_bf16 v[92:95], v[160:163], a[184:187], v[92:95]\n"
    "ds_read2st64_b32 v[214:215], v251 offset0:2 offset1:3\n"
    "v_mfma_f32_16x16x32_bf16 v[92:95], v[164:167], a[188:191], v[92:95]\n"
    "ds_read2st64_b32 v[216:217], v251 offset0:4 offset1:5\n"
    "v_smfmac_f32_16x16x64_bf16 a[204:207], a[192:195], v[160:167], v240\n"
    "ds_read_b128 a[120:123], v203 offset:14336\n"
    "ds_read2st64_b32 v[218:219], v251 offset0:6 offset1:7\n"
    "v_mfma_f32_16x16x32_bf16 v[128:131], a[64:67], a[0:3], v[176:179]\n"
    "v_exp_f32 v152, v152\n"
    "v_mfma_f32_16x16x32_bf16 v[128:131], a[68:71], a[4:7], v[128:131]\n"
    "s_add_u32 m0, s35, 0x1000\n"
    "v_exp_f32 v153, v153\n"
    "v_mfma_f32_16x16x32_bf16 v[128:131], a[72:75], a[8:11], v[128:131]\n"
    "global_load_lds_dwordx4 v241, s[54:55] offset:0\n"
    "v_mfma_f32_16x16x32_bf16 v[128:131], a[76:79], a[12:15], v[128:131]\n"
    "global_load_lds_dwordx4 v241, s[58:59] offset:1024\n"
    "v_cvt_pk_bf16_f32 v172, v152, v153\n"
    "v_xor_b32 v241, 0x2000, v241\n"
    "v_mfma_f32_16x16x32_bf16 v[132:135], a[92:95], a[12:15], v[176:179]\n"
    "v_exp_f32 v154, v154\n"
    "v_mfma_f32_16x16x32_bf16 v[132:135], a[88:91], a[8:11], v[132:135]\n"
    "s_waitcnt lgkmcnt(4)\n"
    "v_exp_f32 v155, v155\n"
    "v_cvt_f32_i32 v204, v204\n"
    "v_mfma_f32_16x16x32_bf16 v[132:135], a[84:87], a[4:7], v[132:135]\n"
    "v_exp_f32 v156, v156\n"
    "v_mfma_f32_16x16x32_bf16 v[132:135], a[80:83], a[0:3], v[132:135]\n"
    "ds_read_b128 a[116:119], v203 offset:13312\n"
    "v_cvt_pk_bf16_f32 v173, v154, v155\n"
    "v_cvt_f32_i32 v205, v205\n"
    "v_mfma_f32_16x16x32_bf16 v[136:139], a[96:99], a[0:3], v[176:179]\n"
    "ds_read_b128 a[112:115], v203 offset:12288\n"
    "v_cvt_f32_i32 v206, v206\n"
    "v_mfma_f32_16x16x32_bf16 v[136:139], a[100:103], a[4:7], v[136:139]\n"
    "v_exp_f32 v157, v157\n"
    "v_mfma_f32_16x16x32_bf16 v[136:139], a[104:107], a[8:11], v[136:139]\n"
    "v_exp_f32 v158, v158\n"
    "v_mfma_f32_16x16x32_bf16 v[136:139], a[108:111], a[12:15], v[136:139]\n"
    "v_exp_f32 v159, v159\n"
    "v_mfma_f32_16x16x32_bf16 v[140:143], a[124:127], a[12:15], v[176:179]\n"
    "s_waitcnt lgkmcnt(2)\n"
    "v_exp_f32 v128, v128\n"
    "v_cvt_f32_i32 v207, v207\n"
    "v_mfma_f32_16x16x32_bf16 v[140:143], a[120:123], a[8:11], v[140:143]\n"
    "s_waitcnt lgkmcnt(1)\n"
    "v_exp_f32 v129, v129\n"
    "v_mfma_f32_16x16x32_bf16 v[140:143], a[116:119], a[4:7], v[140:143]\n"
    "s_waitcnt lgkmcnt(0)\n"
    "v_exp_f32 v130, v130\n"
    "v_cvt_pk_bf16_f32 v174, v156, v157\n"
    "v_cvt_pk_bf16_f32 v175, v158, v159\n"
    "v_mfma_f32_16x16x32_bf16 v[140:143], a[112:115], a[0:3], v[140:143]\n"
    "v_cvt_pk_bf16_f32 v160, v128, v129\n"
    "v_cvt_f32_i32 v208, v208\n"
    "v_mfma_f32_16x16x32_bf16 v[96:99], v[168:171], a[128:131], v[96:99]\n"
    "v_exp_f32 v131, v131\n"
    "v_mfma_f32_16x16x32_bf16 v[96:99], v[172:175], a[132:135], v[96:99]\n"
    "ds_read_b128 a[128:131], v203 offset:16384\n"
    "v_cvt_pk_bf16_f32 v161, v130, v131\n"
    "v_cvt_f32_i32 v209, v209\n"
    "v_mfma_f32_16x16x32_bf16 v[100:103], v[168:171], a[136:139], v[100:103]\n"
    "v_exp_f32 v132, v132\n"
    "v_mfma_f32_16x16x32_bf16 v[100:103], v[172:175], a[140:143], v[100:103]\n"
    "ds_read_b128 a[132:135], v203 offset:24576\n"
    "v_cvt_f32_i32 v210, v210\n"
    "v_mfma_f32_16x16x32_bf16 v[104:107], v[168:171], a[144:147], v[104:107]\n"
    "v_exp_f32 v133, v133\n"
    "v_mfma_f32_16x16x32_bf16 v[104:107], v[172:175], a[148:151], v[104:107]\n"
    "ds_read_b128 a[136:139], v203 offset:17408\n"
    "v_cvt_pk_bf16_f32 v162, v132, v133\n"
    "v_mfma_f32_16x16x32_bf16 v[108:111], v[168:171], a[152:155], v[108:111]\n"
    "v_exp_f32 v134, v134\n"
    "v_cvt_f32_i32 v211, v211\n"
    "v_mfma_f32_16x16x32_bf16 v[108:111], v[172:175], a[156:159], v[108:111]\n"
    "v_fma_f32 v204, v204, v220, v228\n"
    "v_fma_f32 v205, v205, v221, v229\n"
    "v_mfma_f32_16x16x32_bf16 v[112:115], v[168:171], a[160:163], v[112:115]\n"
    "v_exp_f32 v135, v135\n"
    "v_mfma_f32_16x16x32_bf16 v[112:115], v[172:175], a[164:167], v[112:115]\n"
    "ds_read_b128 a[140:143], v203 offset:25600\n"
    "v_cvt_pk_bf16_f32 v163, v134, v135\n"
    "v_mfma_f32_16x16x32_bf16 v[116:119], v[168:171], a[168:171], v[116:119]\n"
    "v_exp_f32 v136, v136\n"
    "v_mfma_f32_16x16x32_bf16 v[116:119], v[172:175], a[172:175], v[116:119]\n"
    "ds_read_b128 a[144:147], v203 offset:18432\n"
    "v_fma_f32 v206, v206, v222, v230\n"
    "v_mfma_f32_16x16x32_bf16 v[120:123], v[168:171], a[176:179], v[120:123]\n"
    "v_exp_f32 v137, v137\n"
    "v_mfma_f32_16x16x32_bf16 v[120:123], v[172:175], a[180:183], v[120:123]\n"
    "ds_read_b128 a[148:151], v203 offset:26624\n"
    "v_cvt_pk_bf16_f32 v164, v136, v137\n"
    "v_mfma_f32_16x16x32_bf16 v[124:127], v[168:171], a[184:187], v[124:127]\n"
    "v_exp_f32 v138, v138\n"
    "v_mfma_f32_16x16x32_bf16 v[124:127], v[172:175], a[188:191], v[124:127]\n"
    "ds_read_b128 a[152:155], v203 offset:19456\n"
    "v_fma_f32 v207, v207, v223, v231\n"
    "v_smfmac_f32_16x16x64_bf16 a[208:211], a[192:195], v[168:175], v240\n"
    "ds_read_b128 a[156:159], v203 offset:27648\n"
    "v_mfma_f32_16x16x32_bf16 v[144:147], a[64:67], a[16:19], v[180:183]\n"
    "v_exp_f32 v139, v139\n"
    "v_mfma_f32_16x16x32_bf16 v[144:147], a[68:71], a[20:23], v[144:147]\n"
    "v_exp_f32 v140, v140\n"
    "v_mfma_f32_16x16x32_bf16 v[144:147], a[72:75], a[24:27], v[144:147]\n"
    "ds_read_b128 a[160:163], v203 offset:20480\n"
    "v_cvt_pk_bf16_f32 v165, v138, v139\n"
    "v_fma_f32 v208, v208, v224, v232\n"
    "v_mfma_f32_16x16x32_bf16 v[144:147], a[76:79], a[28:31], v[144:147]\n"
    "v_exp_f32 v141, v141\n"
    "v_mfma_f32_16x16x32_bf16 v[148:151], a[92:95], a[28:31], v[180:183]\n"
    "ds_read_b128 a[164:167], v203 offset:28672\n"
    "v_cvt_pk_bf16_f32 v166, v140, v141\n"
    "v_fma_f32 v209, v209, v225, v233\n"
    "v_mfma_f32_16x16x32_bf16 v[148:151], a[88:91], a[24:27], v[148:151]\n"
    "v_exp_f32 v142, v142\n"
    "v_mfma_f32_16x16x32_bf16 v[148:151], a[84:87], a[20:23], v[148:151]\n"
    "s_add_u32 m0, s35, 0x1000\n"
    "ds_read_b128 a[168:171], v203 offset:21504\n"
    "v_fma_f32 v210, v210, v226, v234\n"
    "v_mfma_f32_16x16x32_bf16 v[148:151], a[80:83], a[16:19], v[148:151]\n"
    "global_load_lds_dwordx4 v242, s[56:57] offset:2048\n"
    "v_mfma_f32_16x16x32_bf16 v[152:155], a[96:99], a[16:19], v[180:183]\n"
    "global_load_lds_dwordx4 v242, s[84:85] offset:3072\n"
    "v_xor_b32 v242, 0x2000, v242\n"
    "v_mfma_f32_16x16x32_bf16 v[152:155], a[100:103], a[20:23], v[152:155]\n"
    "ds_read_b128 a[172:175], v203 offset:29696\n"
    "v_fma_f32 v211, v211, v227, v235\n"
    "v_mfma_f32_16x16x32_bf16 v[152:155], a[104:107], a[24:27], v[152:155]\n"
    "201: s_cbranch_execz 202f\n"
    "v_exp_f32 v143, v143\n"
    "v_mfma_f32_16x16x32_bf16 v[152:155], a[108:111], a[28:31], v[152:155]\n"
    "ds_read_b128 a[176:179], v203 offset:22528\n"
    "v_cvt_pk_bf16_f32 v167, v142, v143\n"
    "v_mfma_f32_16x16x32_bf16 v[156:159], a[124:127], a[28:31], v[180:183]\n"
    "v_exp_f32 v144, v144\n"
    "v_cvt_pk_bf16_f32 v204, v204, v205\n"
    "v_mfma_f32_16x16x32_bf16 v[156:159], a[120:123], a[24:27], v[156:159]\n"
    "v_exp_f32 v145, v145\n"
    "v_mfma_f32_16x16x32_bf16 v[156:159], a[116:119], a[20:23], v[156:159]\n"
    "ds_read_b128 a[180:183], v203 offset:30720\n"
    "v_cvt_pk_bf16_f32 v168, v144, v145\n"
    "v_cvt_pk_bf16_f32 v205, v206, v207\n"
    "v_mfma_f32_16x16x32_bf16 v[156:159], a[112:115], a[16:19], v[156:159]\n"
    "s_waitcnt lgkmcnt(3)\n"
    "v_exp_f32 v146, v146\n"
    "v_mfma_f32_16x16x32_bf16 v[0:3], v[160:163], a[128:131], v[0:3]\n"
    "ds_read_b128 a[184:187], v203 offset:23552\n"
    "v_cvt_pk_bf16_f32 v206, v208, v209\n"
    "v_mfma_f32_16x16x32_bf16 v[0:3], v[164:167], a[132:135], v[0:3]\n"
    "v_exp_f32 v147, v147\n"
    "v_mfma_f32_16x16x32_bf16 v[4:7], v[160:163], a[136:139], v[4:7]\n"
    "ds_read_b128 a[188:191], v203 offset:31744\n"
    "v_cvt_pk_bf16_f32 v169, v146, v147\n"
    "v_mfma_f32_16x16x32_bf16 v[4:7], v[164:167], a[140:143], v[4:7]\n"
    "v_exp_f32 v148, v148\n"
    "v_cvt_pk_bf16_f32 v207, v210, v211\n"
    "v_mfma_f32_16x16x32_bf16 v[8:11], v[160:163], a[144:147], v[8:11]\n"
    "global_store_dwordx4 v243, v[204:207], s[44:45] sc1\n"
    "v_add_u32 v243, 0x40000, v243\n"
    "v_mfma_f32_16x16x32_bf16 v[8:11], v[164:167], a[148:151], v[8:11]\n"
    "v_exp_f32 v149, v149\n"
    "v_mfma_f32_16x16x32_bf16 v[12:15], v[160:163], a[152:155], v[12:15]\n"
    "v_exp_f32 v150, v150\n"
    "v_cvt_f32_i32 v212, v212\n"
    "v_mfma_f32_16x16x32_bf16 v[12:15], v[164:167], a[156:159], v[12:15]\n"
    "v_cvt_pk_bf16_f32 v170, v148, v149\n"
    "v_cvt_f32_i32 v213, v213\n"
    "v_mfma_f32_16x16x32_bf16 v[16:19], v[160:163], a[160:163], v[16:19]\n"
    "v_exp_f32 v151, v151\n"
    "v_mfma_f32_16x16x32_bf16 v[16:19], v[164:167], a[164:167], v[16:19]\n"
    "v_cvt_pk_bf16_f32 v171, v150, v151\n"
    "v_cvt_f32_i32 v214, v214\n"
    "v_mfma_f32_16x16x32_bf16 v[20:23], v[160:163], a[168:171], v[20:23]\n"
    "s_waitcnt lgkmcnt(1)\n"
    "v_exp_f32 v152, v152\n"
    "v_mfma_f32_16x16x32_bf16 v[20:23], v[164:167], a[172:175], v[20:23]\n"
    "v_cvt_f32_i32 v215, v215\n"
    "v_cvt_f32_i32 v216, v216\n"
    "v_mfma_f32_16x16x32_bf16 v[24:27], v[160:163], a[176:179], v[24:27]\n"
    "v_exp_f32 v153, v153\n"
    "v_mfma_f32_16x16x32_bf16 v[24:27], v[164:167], a[180:183], v[24:27]\n"
    "v_cvt_pk_bf16_f32 v172, v152, v153\n"
    "v_cvt_f32_i32 v217, v217\n"
    "v_mfma_f32_16x16x32_bf16 v[28:31], v[160:163], a[184:187], v[28:31]\n"
    "s_waitcnt lgkmcnt(0)\n"
    "v_exp_f32 v154, v154\n"
    "v_mfma_f32_16x16x32_bf16 v[28:31], v[164:167], a[188:191], v[28:31]\n"
    "v_cvt_f32_i32 v218, v218\n"
    "v_cvt_f32_i32 v219, v219\n"
    "v_smfmac_f32_16x16x64_bf16 a[196:199], a[192:195], v[160:167], v240\n"
    "s_add_u32 m0, s6, 0x10000\n"
    "v_subrev_f32 v212, s16, v212\n"
    "v_mfma_f32_16x16x32_bf16 v[128:131], a[64:67], a[32:35], v[184:187]\n"
    "v_exp_f32 v155, v155\n"
    "v_mfma_f32_16x16x32_bf16 v[128:131], a[68:71], a[36:39], v[128:131]\n"
    "v_exp_f32 v156, v156\n"
    "v_mfma_f32_16x16x32_bf16 v[128:131], a[72:75], a[40:43], v[128:131]\n"
    "s_barrier\n"
    "v_mfma_f32_16x16x32_bf16 v[128:131], a[76:79], a[44:47], v[128:131]\n"
    "global_load_lds_dwordx4 v201, s[40:41] offset:0 sc1\n"
    "v_cvt_pk_bf16_f32 v173, v154, v155\n"
    "v_mfma_f32_16x16x32_bf16 v[132:135], a[92:95], a[44:47], v[184:187]\n"
    "v_exp_f32 v157, v157\n"
    "v_mfma_f32_16x16x32_bf16 v[132:135], a[88:91], a[40:43], v[132:135]\n"
    "v_exp_f32 v158, v158\n"
    "v_mfma_f32_16x16x32_bf16 v[132:135], a[84:87], a[36:39], v[132:135]\n"
    "v_cvt_pk_bf16_f32 v174, v156, v157\n"
    "v_subrev_f32 v213, s17, v213\n"
    "v_mfma_f32_16x16x32_bf16 v[132:135], a[80:83], a[32:35], v[132:135]\n"
    "v_exp_f32 v159, v159\n"
    "v_mfma_f32_16x16x32_bf16 v[136:139], a[96:99], a[32:35], v[184:187]\n"
    "v_cvt_pk_bf16_f32 v175, v158, v159\n"
    "v_subrev_f32 v214, s18, v214\n"
    "v_mfma_f32_16x16x32_bf16 v[136:139], a[100:103], a[36:39], v[136:139]\n"
    "v_subrev_f32 v215, s19, v215\n"
    "v_subrev_f32 v216, s20, v216\n"
    "v_mfma_f32_16x16x32_bf16 v[136:139], a[104:107], a[40:43], v[136:139]\n"
    "global_load_lds_dwordx4 v201, s[40:41] offset:1024 sc1\n"
    "v_mfma_f32_16x16x32_bf16 v[136:139], a[108:111], a[44:47], v[136:139]\n"
    "v_exp_f32 v128, v128\n"
    "v_mfma_f32_16x16x32_bf16 v[140:143], a[124:127], a[44:47], v[184:187]\n"
    "v_exp_f32 v129, v129\n"
    "v_mfma_f32_16x16x32_bf16 v[140:143], a[120:123], a[40:43], v[140:143]\n"
    "v_cvt_pk_bf16_f32 v160, v128, v129\n"
    "v_subrev_f32 v217, s21, v217\n"
    "v_mfma_f32_16x16x32_bf16 v[140:143], a[116:119], a[36:39], v[140:143]\n"
    "v_exp_f32 v130, v130\n"
    "v_mfma_f32_16x16x32_bf16 v[140:143], a[112:115], a[32:35], v[140:143]\n"
    "v_subrev_f32 v218, s22, v218\n"
    "v_subrev_f32 v219, s23, v219\n"
    "v_mfma_f32_16x16x32_bf16 v[32:35], v[168:171], a[128:131], v[32:35]\n"
    "v_exp_f32 v131, v131\n"
    "v_mfma_f32_16x16x32_bf16 v[32:35], v[172:175], a[132:135], v[32:35]\n"
    "global_load_lds_dwordx4 v201, s[40:41] offset:2048 sc1\n"
    "v_mfma_f32_16x16x32_bf16 v[36:39], v[168:171], a[136:139], v[36:39]\n"
    "global_load_lds_dwordx4 v201, s[40:41] offset:3072 sc1\n"
    "v_cvt_pk_bf16_f32 v161, v130, v131\n"
    "v_mfma_f32_16x16x32_bf16 v[36:39], v[172:175], a[140:143], v[36:39]\n"
    "s_add_u32 m0, s6, 0x11000\n"
    "v_exp_f32 v132, v132\n"
    "v_mfma_f32_16x16x32_bf16 v[40:43], v[168:171], a[144:147], v[40:43]\n"
    "v_exp_f32 v133, v133\n"
    "v_mfma_f32_16x16x32_bf16 v[40:43], v[172:175], a[148:151], v[40:43]\n"
    "v_cvt_pk_bf16_f32 v162, v132, v133\n"
    "v_mul_f32 v212, s8, v212\n"
    "v_mfma_f32_16x16x32_bf16 v[44:47], v[168:171], a[152:155], v[44:47]\n"
    "v_exp_f32 v134, v134\n"
    "v_mfma_f32_16x16x32_bf16 v[44:47], v[172:175], a[156:159], v[44:47]\n"
    "v_mul_f32 v213, s9, v213\n"
    "v_mul_f32 v214, s10, v214\n"
    "v_mfma_f32_16x16x32_bf16 v[48:51], v[168:171], a[160:163], v[48:51]\n"
    "v_exp_f32 v135, v135\n"
    "v_mfma_f32_16x16x32_bf16 v[48:51], v[172:175], a[164:167], v[48:51]\n"
    "global_load_lds_dwordx4 v202, s[40:41] offset:0 sc1\n"
    "v_mfma_f32_16x16x32_bf16 v[52:55], v[168:171], a[168:171], v[52:55]\n"
    "v_exp_f32 v136, v136\n"
    "v_mfma_f32_16x16x32_bf16 v[52:55], v[172:175], a[172:175], v[52:55]\n"
    "v_cvt_pk_bf16_f32 v163, v134, v135\n"
    "v_mul_f32 v215, s11, v215\n"
    "v_mfma_f32_16x16x32_bf16 v[56:59], v[168:171], a[176:179], v[56:59]\n"
    "v_exp_f32 v137, v137\n"
    "v_mfma_f32_16x16x32_bf16 v[56:59], v[172:175], a[180:183], v[56:59]\n"
    "v_cvt_pk_bf16_f32 v164, v136, v137\n"
    "v_mul_f32 v216, s12, v216\n"
    "v_mfma_f32_16x16x32_bf16 v[60:63], v[168:171], a[184:187], v[60:63]\n"
    "v_exp_f32 v138, v138\n"
    "v_mfma_f32_16x16x32_bf16 v[60:63], v[172:175], a[188:191], v[60:63]\n"
    "v_mul_f32 v217, s13, v217\n"
    "v_mul_f32 v218, s14, v218\n"
    "v_smfmac_f32_16x16x64_bf16 a[200:203], a[192:195], v[168:175], v240\n"
    "v_mul_f32 v219, s15, v219\n"
    "v_mfma_f32_16x16x32_bf16 v[144:147], a[64:67], a[48:51], v[188:191]\n"
    "global_load_lds_dwordx4 v202, s[40:41] offset:1024 sc1\n"
    "v_mfma_f32_16x16x32_bf16 v[144:147], a[68:71], a[52:55], v[144:147]\n"
    "v_exp_f32 v139, v139\n"
    "v_mfma_f32_16x16x32_bf16 v[144:147], a[72:75], a[56:59], v[144:147]\n"
    "ds_read_b128 a[64:67], v201 offset:0\n"
    "v_cvt_pk_bf16_f32 v165, v138, v139\n"
    "v_mfma_f32_16x16x32_bf16 v[144:147], a[76:79], a[60:63], v[144:147]\n"
    "v_exp_f32 v140, v140\n"
    "v_mfma_f32_16x16x32_bf16 v[148:151], a[92:95], a[60:63], v[188:191]\n"
    "ds_read_b128 a[68:71], v201 offset:1024\n"
    "v_cvt_pk_bf16_f32 v212, v212, v213\n"
    "v_mfma_f32_16x16x32_bf16 v[148:151], a[88:91], a[56:59], v[148:151]\n"
    "v_exp_f32 v141, v141\n"
    "v_mfma_f32_16x16x32_bf16 v[148:151], a[84:87], a[52:55], v[148:151]\n"
    "ds_read_b128 a[72:75], v201 offset:2048\n"
    "v_cvt_pk_bf16_f32 v166, v140, v141\n"
    "v_mfma_f32_16x16x32_bf16 v[148:151], a[80:83], a[48:51], v[148:151]\n"
    "global_load_lds_dwordx4 v202, s[40:41] offset:2048 sc1\n"
    "v_mfma_f32_16x16x32_bf16 v[152:155], a[96:99], a[48:51], v[188:191]\n"
    "v_exp_f32 v142, v142\n"
    "v_mfma_f32_16x16x32_bf16 v[152:155], a[100:103], a[52:55], v[152:155]\n"
    "ds_read_b128 a[76:79], v201 offset:3072\n"
    "v_cvt_pk_bf16_f32 v213, v214, v215\n"
    "v_mfma_f32_16x16x32_bf16 v[152:155], a[104:107], a[56:59], v[152:155]\n"
    "v_exp_f32 v143, v143\n"
    "v_mfma_f32_16x16x32_bf16 v[152:155], a[108:111], a[60:63], v[152:155]\n"
    "ds_read_b128 a[92:95], v201 offset:7168\n"
    "v_cvt_pk_bf16_f32 v167, v142, v143\n"
    "v_mfma_f32_16x16x32_bf16 v[156:159], a[124:127], a[60:63], v[188:191]\n"
    "v_exp_f32 v144, v144\n"
    "v_mfma_f32_16x16x32_bf16 v[156:159], a[120:123], a[56:59], v[156:159]\n"
    "ds_read_b128 a[88:91], v201 offset:6144\n"
    "v_cvt_pk_bf16_f32 v214, v216, v217\n"
    "v_mfma_f32_16x16x32_bf16 v[156:159], a[116:119], a[52:55], v[156:159]\n"
    "global_load_lds_dwordx4 v202, s[40:41] offset:3072 sc1\n"
    "v_mfma_f32_16x16x32_bf16 v[156:159], a[112:115], a[48:51], v[156:159]\n"
    "s_add_u32 s40, s40, 0x8000\n"
    "v_exp_f32 v145, v145\n"
    "v_mfma_f32_16x16x32_bf16 v[64:67], v[160:163], a[128:131], v[64:67]\n"
    "s_addc_u32 s41, s41, 0\n"
    "ds_read_b128 a[84:87], v201 offset:5120\n"
    "v_cvt_pk_bf16_f32 v168, v144, v145\n"
    "v_mfma_f32_16x16x32_bf16 v[64:67], v[164:167], a[132:135], v[64:67]\n"
    "v_exp_f32 v146, v146\n"
    "v_mfma_f32_16x16x32_bf16 v[68:71], v[160:163], a[136:139], v[68:71]\n"
    "ds_read_b128 a[80:83], v201 offset:4096\n"
    "v_cvt_pk_bf16_f32 v215, v218, v219\n"
    "v_mfma_f32_16x16x32_bf16 v[68:71], v[164:167], a[140:143], v[68:71]\n"
    "global_store_dwordx4 v244, v[212:215], s[44:45] sc1\n"
    "v_add_u32 v244, 0x40000, v244\n"
    "v_mfma_f32_16x16x32_bf16 v[72:75], v[160:163], a[144:147], v[72:75]\n"
    "v_exp_f32 v147, v147\n"
    "v_mfma_f32_16x16x32_bf16 v[72:75], v[164:167], a[148:151], v[72:75]\n"
    "v_exp_f32 v148, v148\n"
    "v_mfma_f32_16x16x32_bf16 v[76:79], v[160:163], a[152:155], v[76:79]\n"
    "ds_read_b128 a[96:99], v201 offset:8192\n"
    "v_cvt_pk_bf16_f32 v169, v146, v147\n"
    "v_mfma_f32_16x16x32_bf16 v[76:79], v[164:167], a[156:159], v[76:79]\n"
    "v_exp_f32 v149, v149\n"
    "v_mfma_f32_16x16x32_bf16 v[80:83], v[160:163], a[160:163], v[80:83]\n"
    "ds_read_b128 a[100:103], v201 offset:9216\n"
    "v_cvt_pk_bf16_f32 v170, v148, v149\n"
    "v_mfma_f32_16x16x32_bf16 v[80:83], v[164:167], a[164:167], v[80:83]\n"
    "v_exp_f32 v150, v150\n"
    "v_mfma_f32_16x16x32_bf16 v[84:87], v[160:163], a[168:171], v[84:87]\n"
    "ds_read_b128 a[104:107], v201 offset:10240\n"
    "v_mfma_f32_16x16x32_bf16 v[84:87], v[164:167], a[172:175], v[84:87]\n"
    "v_exp_f32 v151, v151\n"
    "v_mfma_f32_16x16x32_bf16 v[88:91], v[160:163], a[176:179], v[88:91]\n"
    "ds_read_b128 a[108:111], v201 offset:11264\n"
    "v_cvt_pk_bf16_f32 v171, v150, v151\n"
    "v_mfma_f32_16x16x32_bf16 v[88:91], v[164:167], a[180:183], v[88:91]\n"
    "v_exp_f32 v152, v152\n"
    "v_mfma_f32_16x16x32_bf16 v[92:95], v[160:163], a[184:187], v[92:95]\n"
    "ds_read_b128 a[124:127], v201 offset:15360\n"
    "v_mfma_f32_16x16x32_bf16 v[92:95], v[164:167], a[188:191], v[92:95]\n"
    "v_exp_f32 v153, v153\n"
    "v_smfmac_f32_16x16x64_bf16 a[204:207], a[192:195], v[160:167], v240\n"
    "s_waitcnt lgkmcnt(4)\n"
    "ds_read_b128 a[120:123], v201 offset:14336\n"
    "v_mfma_f32_16x16x32_bf16 v[128:131], a[64:67], a[0:3], v[176:179]\n"
    "v_exp_f32 v154, v154\n"
    "v_mfma_f32_16x16x32_bf16 v[128:131], a[68:71], a[4:7], v[128:131]\n"
    "ds_read_b128 a[116:119], v201 offset:13312\n"
    "v_cvt_pk_bf16_f32 v172, v152, v153\n"
    "v_mfma_f32_16x16x32_bf16 v[128:131], a[72:75], a[8:11], v[128:131]\n"
    "v_exp_f32 v155, v155\n"
    "v_mfma_f32_16x16x32_bf16 v[128:131], a[76:79], a[12:15], v[128:131]\n"
    "ds_read_b128 a[112:115], v201 offset:12288\n"
    "v_cvt_pk_bf16_f32 v173, v154, v155\n"
    "v_mfma_f32_16x16x32_bf16 v[132:135], a[92:95], a[12:15], v[176:179]\n"
    "v_exp_f32 v156, v156\n"
    "v_mfma_f32_16x16x32_bf16 v[132:135], a[88:91], a[8:11], v[132:135]\n"
    "v_exp_f32 v157, v157\n"
    "v_mfma_f32_16x16x32_bf16 v[132:135], a[84:87], a[4:7], v[132:135]\n"
    "v_exp_f32 v158, v158\n"
    "v_mfma_f32_16x16x32_bf16 v[132:135], a[80:83], a[0:3], v[132:135]\n"
    "v_exp_f32 v159, v159\n"
    "v_mfma_f32_16x16x32_bf16 v[136:139], a[96:99], a[0:3], v[176:179]\n"
    "s_waitcnt lgkmcnt(2)\n"
    "v_cvt_pk_bf16_f32 v174, v156, v157\n"
    "v_cvt_pk_bf16_f32 v175, v158, v159\n"
    "v_mfma_f32_16x16x32_bf16 v[136:139], a[100:103], a[4:7], v[136:139]\n"
    "v_mfma_f32_16x16x32_bf16 v[136:139], a[104:107], a[8:11], v[136:139]\n"
    "v_exp_f32 v128, v128\n"
    "v_mfma_f32_16x16x32_bf16 v[136:139], a[108:111], a[12:15], v[136:139]\n"
    "v_exp_f32 v129, v129\n"
    "v_mfma_f32_16x16x32_bf16 v[140:143], a[124:127], a[12:15], v[176:179]\n"
    "v_cvt_pk_bf16_f32 v160, v128, v129\n"
    "v_mfma_f32_16x16x32_bf16 v[140:143], a[120:123], a[8:11], v[140:143]\n"
    "s_waitcnt lgkmcnt(0)\n"
    "v_exp_f32 v130, v130\n"
    "v_mfma_f32_16x16x32_bf16 v[140:143], a[116:119], a[4:7], v[140:143]\n"
    "v_exp_f32 v131, v131\n"
    "v_mfma_f32_16x16x32_bf16 v[140:143], a[112:115], a[0:3], v[140:143]\n"
    "v_cvt_pk_bf16_f32 v161, v130, v131\n"
    "v_mfma_f32_16x16x32_bf16 v[96:99], v[168:171], a[128:131], v[96:99]\n"
    "v_exp_f32 v132, v132\n"
    "v_mfma_f32_16x16x32_bf16 v[96:99], v[172:175], a[132:135], v[96:99]\n"
    "ds_read_b128 a[128:131], v201 offset:16384\n"
    "v_mfma_f32_16x16x32_bf16 v[100:103], v[168:171], a[136:139], v[100:103]\n"
    "v_exp_f32 v133, v133\n"
    "v_mfma_f32_16x16x32_bf16 v[100:103], v[172:175], a[140:143], v[100:103]\n"
    "ds_read_b128 a[132:135], v201 offset:24576\n"
    "v_cvt_pk_bf16_f32 v162, v132, v133\n"
    "v_mfma_f32_16x16x32_bf16 v[104:107], v[168:171], a[144:147], v[104:107]\n"
    "s_waitcnt vmcnt(12)\n"
    "ds_read_b128 v[204:207], v249 offset:4096\n"
    "v_mfma_f32_16x16x32_bf16 v[104:107], v[172:175], a[148:151], v[104:107]\n"
    "ds_read_b128 a[136:139], v201 offset:17408\n"
    "v_mfma_f32_16x16x32_bf16 v[108:111], v[168:171], a[152:155], v[108:111]\n"
    "ds_read_b128 v[208:211], v249 offset:4112\n"
    "v_mfma_f32_16x16x32_bf16 v[108:111], v[172:175], a[156:159], v[108:111]\n"
    "s_waitcnt vmcnt(10)\n"
    "ds_read2st64_b32 v[212:213], v251 offset0:16 offset1:17\n"
    "v_mfma_f32_16x16x32_bf16 v[112:115], v[168:171], a[160:163], v[112:115]\n"
    "ds_read2st64_b32 v[214:215], v251 offset0:18 offset1:19\n"
    "v_mfma_f32_16x16x32_bf16 v[112:115], v[172:175], a[164:167], v[112:115]\n"
    "v_exp_f32 v134, v134\n"
    "v_mfma_f32_16x16x32_bf16 v[116:119], v[168:171], a[168:171], v[116:119]\n"
    "ds_read_b128 a[140:143], v201 offset:25600\n"
    "ds_read2st64_b32 v[216:217], v251 offset0:20 offset1:21\n"
    "v_mfma_f32_16x16x32_bf16 v[116:119], v[172:175], a[172:175], v[116:119]\n"
    "ds_read2st64_b32 v[218:219], v251 offset0:22 offset1:23\n"
    "v_mfma_f32_16x16x32_bf16 v[120:123], v[168:171], a[176:179], v[120:123]\n"
    "v_exp_f32 v135, v135\n"
    "v_mfma_f32_16x16x32_bf16 v[120:123], v[172:175], a[180:183], v[120:123]\n"
    "v_exp_f32 v136, v136\n"
    "v_mfma_f32_16x16x32_bf16 v[124:127], v[168:171], a[184:187], v[124:127]\n"
    "ds_read_b128 a[144:147], v201 offset:18432\n"
    "v_cvt_pk_bf16_f32 v163, v134, v135\n"
    "v_mfma_f32_16x16x32_bf16 v[124:127], v[172:175], a[188:191], v[124:127]\n"
    "v_exp_f32 v137, v137\n"
    "v_smfmac_f32_16x16x64_bf16 a[208:211], a[192:195], v[168:175], v240\n"
    "ds_read_b128 a[148:151], v201 offset:26624\n"
    "v_mfma_f32_16x16x32_bf16 v[144:147], a[64:67], a[16:19], v[180:183]\n"
    "s_waitcnt lgkmcnt(5)\n"
    "v_exp_f32 v138, v138\n"
    "v_cvt_f32_i32 v204, v204\n"
    "v_mfma_f32_16x16x32_bf16 v[144:147], a[68:71], a[20:23], v[144:147]\n"
    "ds_read_b128 a[152:155], v201 offset:19456\n"
    "v_cvt_pk_bf16_f32 v164, v136, v137\n"
    "v_mfma_f32_16x16x32_bf16 v[144:147], a[72:75], a[24:27], v[144:147]\n"
    "v_exp_f32 v139, v139\n"
    "v_cvt_f32_i32 v205, v205\n"
    "v_mfma_f32_16x16x32_bf16 v[144:147], a[76:79], a[28:31], v[144:147]\n"
    "ds_read_b128 a[156:159], v201 offset:27648\n"
    "v_cvt_pk_bf16_f32 v165, v138, v139\n"
    "v_mfma_f32_16x16x32_bf16 v[148:151], a[92:95], a[28:31], v[180:183]\n"
    "v_exp_f32 v140, v140\n"
    "v_mfma_f32_16x16x32_bf16 v[148:151], a[88:91], a[24:27], v[148:151]\n"
    "ds_read_b128 a[160:163], v201 offset:20480\n"
    "v_cvt_f32_i32 v206, v206\n"
    "v_mfma_f32_16x16x32_bf16 v[148:151], a[84:87], a[20:23], v[148:151]\n"
    "v_exp_f32 v141, v141\n"
    "v_mfma_f32_16x16x32_bf16 v[148:151], a[80:83], a[16:19], v[148:151]\n"
    "ds_read_b128 a[164:167], v201 offset:28672\n"
    "v_cvt_pk_bf16_f32 v166, v140, v141\n"
    "v_cvt_f32_i32 v207, v207\n"
    "v_mfma_f32_16x16x32_bf16 v[152:155], a[96:99], a[16:19], v[180:183]\n"
    "v_exp_f32 v142, v142\n"
    "v_mfma_f32_16x16x32_bf16 v[152:155], a[100:103], a[20:23], v[152:155]\n"
    "ds_read_b128 a[168:171], v201 offset:21504\n"
    "v_cvt_f32_i32 v208, v208\n"
    "v_mfma_f32_16x16x32_bf16 v[152:155], a[104:107], a[24:27], v[152:155]\n"
    "v_exp_f32 v143, v143\n"
    "v_mfma_f32_16x16x32_bf16 v[152:155], a[108:111], a[28:31], v[152:155]\n"
    "ds_read_b128 a[172:175], v201 offset:29696\n"
    "v_cvt_pk_bf16_f32 v167, v142, v143\n"
    "v_mfma_f32_16x16x32_bf16 v[156:159], a[124:127], a[28:31], v[180:183]\n"
    "ds_read_b128 a[176:179], v201 offset:22528\n"
    "v_cvt_f32_i32 v209, v209\n"
    "v_mfma_f32_16x16x32_bf16 v[156:159], a[120:123], a[24:27], v[156:159]\n"
    "v_exp_f32 v144, v144\n"
    "v_mfma_f32_16x16x32_bf16 v[156:159], a[116:119], a[20:23], v[156:159]\n"
    "ds_read_b128 a[180:183], v201 offset:30720\n"
    "v_cvt_f32_i32 v210, v210\n"
    "v_mfma_f32_16x16x32_bf16 v[156:159], a[112:115], a[16:19], v[156:159]\n"
    "v_exp_f32 v145, v145\n"
    "v_mfma_f32_16x16x32_bf16 v[0:3], v[160:163], a[128:131], v[0:3]\n"
    "ds_read_b128 a[184:187], v201 offset:23552\n"
    "v_cvt_pk_bf16_f32 v168, v144, v145\n"
    "v_mfma_f32_16x16x32_bf16 v[0:3], v[164:167], a[132:135], v[0:3]\n"
    "v_exp_f32 v146, v146\n"
    "v_cvt_f32_i32 v211, v211\n"
    "v_mfma_f32_16x16x32_bf16 v[4:7], v[160:163], a[136:139], v[4:7]\n"
    "s_waitcnt lgkmcnt(4)\n"
    "ds_read_b128 a[188:191], v201 offset:31744\n"
    "v_fma_f32 v204, v204, v220, v228\n"
    "v_mfma_f32_16x16x32_bf16 v[4:7], v[164:167], a[140:143], v[4:7]\n"
    "v_exp_f32 v147, v147\n"
    "v_mfma_f32_16x16x32_bf16 v[8:11], v[160:163], a[144:147], v[8:11]\n"
    "v_exp_f32 v148, v148\n"
    "v_mfma_f32_16x16x32_bf16 v[8:11], v[164:167], a[148:151], v[8:11]\n"
    "v_exp_f32 v149, v149\n"
    "v_mfma_f32_16x16x32_bf16 v[12:15], v[160:163], a[152:155], v[12:15]\n"
    "v_exp_f32 v150, v150\n"
    "v_fma_f32 v205, v205, v221, v229\n"
    "v_mfma_f32_16x16x32_bf16 v[12:15], v[164:167], a[156:159], v[12:15]\n"
    "v_cvt_pk_bf16_f32 v169, v146, v147\n"
    "v_cvt_pk_bf16_f32 v170, v148, v149\n"
    "v_mfma_f32_16x16x32_bf16 v[16:19], v[160:163], a[160:163], v[16:19]\n"
    "v_exp_f32 v151, v151\n"
    "v_mfma_f32_16x16x32_bf16 v[16:19], v[164:167], a[164:167], v[16:19]\n"
    "v_cvt_pk_bf16_f32 v171, v150, v151\n"
    "v_fma_f32 v206, v206, v222, v230\n"
    "v_mfma_f32_16x16x32_bf16 v[20:23], v[160:163], a[168:171], v[20:23]\n"
    "s_waitcnt lgkmcnt(1)\n"
    "v_exp_f32 v152, v152\n"
    "v_mfma_f32_16x16x32_bf16 v[20:23], v[164:167], a[172:175], v[20:23]\n"
    "v_fma_f32 v207, v207, v223, v231\n"
    "v_fma_f32 v208, v208, v224, v232\n"
    "v_mfma_f32_16x16x32_bf16 v[24:27], v[160:163], a[176:179], v[24:27]\n"
    "v_exp_f32 v153, v153\n"
    "v_mfma_f32_16x16x32_bf16 v[24:27], v[164:167], a[180:183], v[24:27]\n"
    "v_cvt_pk_bf16_f32 v172, v152, v153\n"
    "v_fma_f32 v209, v209, v225, v233\n"
    "v_mfma_f32_16x16x32_bf16 v[28:31], v[160:163], a[184:187], v[28:31]\n"
    "s_waitcnt lgkmcnt(0)\n"
    "v_exp_f32 v154, v154\n"
    "v_mfma_f32_16x16x32_bf16 v[28:31], v[164:167], a[188:191], v[28:31]\n"
    "v_fma_f32 v210, v210, v226, v234\n"
    "v_fma_f32 v211, v211, v227, v235\n"
    "v_smfmac_f32_16x16x64_bf16 a[196:199], a[192:195], v[160:167], v240\n"
    "s_add_u32 m0, s6, 0x0\n"
    "v_cvt_pk_bf16_f32 v204, v204, v205\n"
    "v_mfma_f32_16x16x32_bf16 v[128:131], a[64:67], a[32:35], v[184:187]\n"
    "v_exp_f32 v155, v155\n"
    "v_mfma_f32_16x16x32_bf16 v[128:131], a[68:71], a[36:39], v[128:131]\n"
    "s_waitcnt vmcnt(0)\n"
    "v_exp_f32 v156, v156\n"
    "v_mfma_f32_16x16x32_bf16 v[128:131], a[72:75], a[40:43], v[128:131]\n"
    "s_barrier\n"
    "s_cmp_lg_u32 %[w], 0\n"
    "s_cbranch_scc1 26f\n"
    "global_store_dwordx2 v248, v[246:247], s[66:67] offset:256 sc1\n"
    "26:\n"
    "v_mfma_f32_16x16x32_bf16 v[128:131], a[76:79], a[44:47], v[128:131]\n"
    "global_load_lds_dwordx4 v201, s[40:41] offset:0 sc1\n"
    "v_cvt_pk_bf16_f32 v173, v154, v155\n"
    "v_mfma_f32_16x16x32_bf16 v[132:135], a[92:95], a[44:47], v[184:187]\n"
    "v_exp_f32 v157, v157\n"
    "v_mfma_f32_16x16x32_bf16 v[132:135], a[88:91], a[40:43], v[132:135]\n"
    "v_exp_f32 v158, v158\n"
    "v_mfma_f32_16x16x32_bf16 v[132:135], a[84:87], a[36:39], v[132:135]\n"
    "v_cvt_pk_bf16_f32 v174, v156, v157\n"
    "v_cvt_pk_bf16_f32 v205, v206, v207\n"
    "v_mfma_f32_16x16x32_bf16 v[132:135], a[80:83], a[32:35], v[132:135]\n"
    "v_exp_f32 v159, v159\n"
    "v_mfma_f32_16x16x32_bf16 v[136:139], a[96:99], a[32:35], v[184:187]\n"
    "v_cvt_pk_bf16_f32 v175, v158, v159\n"
    "v_cvt_pk_bf16_f32 v206, v208, v209\n"
    "v_mfma_f32_16x16x32_bf16 v[136:139], a[100:103], a[36:39], v[136:139]\n"
    "v_cvt_pk_bf16_f32 v207, v210, v211\n"
    "v_cvt_f32_i32 v212, v212\n"
    "v_mfma_f32_16x16x32_bf16 v[136:139], a[104:107], a[40:43], v[136:139]\n"
    "global_load_lds_dwordx4 v201, s[40:41] offset:1024 sc1\n"
    "v_mfma_f32_16x16x32_bf16 v[136:139], a[108:111], a[44:47], v[136:139]\n"
    "v_exp_f32 v128, v128\n"
    "v_mfma_f32_16x16x32_bf16 v[140:143], a[124:127], a[44:47], v[184:187]\n"
    "v_exp_f32 v129, v129\n"
    "v_mfma_f32_16x16x32_bf16 v[140:143], a[120:123], a[40:43], v[140:143]\n"
    "global_store_dwordx4 v243, v[204:207], s[44:45] sc1\n"
    "v_cvt_pk_bf16_f32 v160, v128, v129\n"
    "v_add_u32 v243, 0x40000, v243\n"
    "v_mfma_f32_16x16x32_bf16 v[140:143], a[116:119], a[36:39], v[140:143]\n"
    "v_exp_f32 v130, v130\n"
    "v_mfma_f32_16x16x32_bf16 v[140:143], a[112:115], a[32:35], v[140:143]\n"
    "v_cvt_f32_i32 v213, v213\n"
    "v_cvt_f32_i32 v214, v214\n"
    "v_mfma_f32_16x16x32_bf16 v[32:35], v[168:171], a[128:131], v[32:35]\n"
    "global_load_lds_dwordx4 v201, s[40:41] offset:2048 sc1\n"
    "v_mfma_f32_16x16x32_bf16 v[32:35], v[172:175], a[132:135], v[32:35]\n"
    "v_exp_f32 v131, v131\n"
    "v_mfma_f32_16x16x32_bf16 v[36:39], v[168:171], a[136:139], v[36:39]\n"
    "v_exp_f32 v132, v132\n"
    "v_mfma_f32_16x16x32_bf16 v[36:39], v[172:175], a[140:143], v[36:39]\n"
    "v_cvt_pk_bf16_f32 v161, v130, v131\n"
    "v_cvt_f32_i32 v215, v215\n"
    "v_mfma_f32_16x16x32_bf16 v[40:43], v[168:171], a[144:147], v[40:43]\n"
    "v_exp_f32 v133, v133\n"
    "v_mfma_f32_16x16x32_bf16 v[40:43], v[172:175], a[148:151], v[40:43]\n"
    "v_cvt_pk_bf16_f32 v162, v132, v133\n"
    "v_cvt_f32_i32 v216, v216\n"
    "v_mfma_f32_16x16x32_bf16 v[44:47], v[168:171], a[152:155], v[44:47]\n"
    "v_exp_f32 v134, v134\n"
    "v_mfma_f32_16x16x32_bf16 v[44:47], v[172:175], a[156:159], v[44:47]\n"
    "global_load_lds_dwordx4 v201, s[40:41] offset:3072 sc1\n"
    "s_add_u32 m0, s6, 0x1000\n"
    "v_mfma_f32_16x16x32_bf16 v[48:51], v[168:171], a[160:163], v[48:51]\n"
    "global_load_lds_dwordx4 v202, s[40:41] offset:0 sc1\n"
    "v_cvt_f32_i32 v217, v217\n"
    "v_mfma_f32_16x16x32_bf16 v[48:51], v[172:175], a[164:167], v[48:51]\n"
    "v_exp_f32 v135, v135\n"
    "v_mfma_f32_16x16x32_bf16 v[52:55], v[168:171], a[168:171], v[52:55]\n"
    "v_exp_f32 v136, v136\n"
    "v_mfma_f32_16x16x32_bf16 v[52:55], v[172:175], a[172:175], v[52:55]\n"
    "v_cvt_pk_bf16_f32 v163, v134, v135\n"
    "v_cvt_f32_i32 v218, v218\n"
    "v_mfma_f32_16x16x32_bf16 v[56:59], v[168:171], a[176:179], v[56:59]\n"
    "v_exp_f32 v137, v137\n"
    "v_mfma_f32_16x16x32_bf16 v[56:59], v[172:175], a[180:183], v[56:59]\n"
    "v_cvt_pk_bf16_f32 v164, v136, v137\n"
    "v_cvt_f32_i32 v219, v219\n"
    "v_mfma_f32_16x16x32_bf16 v[60:63], v[168:171], a[184:187], v[60:63]\n"
    "v_exp_f32 v138, v138\n"
    "v_mfma_f32_16x16x32_bf16 v[60:63], v[172:175], a[188:191], v[60:63]\n"
    "global_load_lds_dwordx4 v202, s[40:41] offset:1024 sc1\n"
    "v_smfmac_f32_16x16x64_bf16 a[200:203], a[192:195], v[168:175], v240\n"
    "v_subrev_f32 v212, s68, v212\n"
    "v_mfma_f32_16x16x32_bf16 v[144:147], a[64:67], a[48:51], v[188:191]\n"
    "v_exp_f32 v139, v139\n"
    "v_mfma_f32_16x16x32_bf16 v[144:147], a[68:71], a[52:55], v[144:147]\n"
    "v_exp_f32 v140, v140\n"
    "v_mfma_f32_16x16x32_bf16 v[144:147], a[72:75], a[56:59], v[144:147]\n"
    "ds_read_b128 a[64:67], v201 offset:32768\n"
    "v_cvt_pk_bf16_f32 v165, v138, v139\n"
    "v_mfma_f32_16x16x32_bf16 v[144:147], a[76:79], a[60:63], v[144:147]\n"
    "v_exp_f32 v141, v141\n"
    "v_mfma_f32_16x16x32_bf16 v[148:151], a[92:95], a[60:63], v[188:191]\n"
    "ds_read_b128 a[68:71], v201 offset:33792\n"
    "v_cvt_pk_bf16_f32 v166, v140, v141\n"
    "v_mfma_f32_16x16x32_bf16 v[148:151], a[88:91], a[56:59], v[148:151]\n"
    "global_load_lds_dwordx4 v202, s[40:41] offset:2048 sc1\n"
    "v_mfma_f32_16x16x32_bf16 v[148:151], a[84:87], a[52:55], v[148:151]\n"
    "ds_read_b128 a[72:75], v201 offset:34816\n"
    "v_subrev_f32 v213, s69, v213\n"
    "v_mfma_f32_16x16x32_bf16 v[148:151], a[80:83], a[48:51], v[148:151]\n"
    "v_exp_f32 v142, v142\n"
    "v_mfma_f32_16x16x32_bf16 v[152:155], a[96:99], a[48:51], v[188:191]\n"
    "v_exp_f32 v143, v143\n"
    "v_mfma_f32_16x16x32_bf16 v[152:155], a[100:103], a[52:55], v[152:155]\n"
    "ds_read_b128 a[76:79], v201 offset:35840\n"
    "v_cvt_pk_bf16_f32 v167, v142, v143\n"
    "v_subrev_f32 v214, s70, v214\n"
    "v_mfma_f32_16x16x32_bf16 v[152:155], a[104:107], a[56:59], v[152:155]\n"
    "v_exp_f32 v144, v144\n"
    "v_mfma_f32_16x16x32_bf16 v[152:155], a[108:111], a[60:63], v[152:155]\n"
    "ds_read_b128 a[92:95], v201 offset:39936\n"
    "v_subrev_f32 v215, s71, v215\n"
    "v_mfma_f32_16x16x32_bf16 v[156:159], a[124:127], a[60:63], v[188:191]\n"
    "global_load_lds_dwordx4 v202, s[40:41] offset:3072 sc1\n"
    "v_mfma_f32_16x16x32_bf16 v[156:159], a[120:123], a[56:59], v[156:159]\n"
    "s_add_u32 s40, s40, 0x8000\n"
    "ds_read_b128 a[88:91], v201 offset:38912\n"
    "v_subrev_f32 v216, s72, v216\n"
    "v_mfma_f32_16x16x32_bf16 v[156:159], a[116:119], a[52:55], v[156:159]\n"
    "s_addc_u32 s41, s41, 0\n"
    "v_exp_f32 v145, v145\n"
    "v_mfma_f32_16x16x32_bf16 v[156:159], a[112:115], a[48:51], v[156:159]\n"
    "ds_read_b128 a[84:87], v201 offset:37888\n"
    "v_cvt_pk_bf16_f32 v168, v144, v145\n"
    "v_mfma_f32_16x16x32_bf16 v[64:67], v[160:163], a[128:131], v[64:67]\n"
    "s_add_u32 m0, s35, 0x2000\n"
    "v_exp_f32 v146, v146\n"
    "v_subrev_f32 v217, s73, v217\n"
    "v_mfma_f32_16x16x32_bf16 v[64:67], v[164:167], a[132:135], v[64:67]\n"
    "ds_read_b128 a[80:83], v201 offset:36864\n"
    "v_subrev_f32 v218, s74, v218\n"
    "v_mfma_f32_16x16x32_bf16 v[68:71], v[160:163], a[136:139], v[68:71]\n"
    "v_exp_f32 v147, v147\n"
    "v_mfma_f32_16x16x32_bf16 v[68:71], v[164:167], a[140:143], v[68:71]\n"
    "global_load_lds_dwordx4 v241, s[54:55] offset:0\n"
    "v_mfma_f32_16x16x32_bf16 v[72:75], v[160:163], a[144:147], v[72:75]\n"
    "global_load_lds_dwordx4 v241, s[58:59] offset:1024\n"
    "v_cvt_pk_bf16_f32 v169, v146, v147\n"
    "v_mfma_f32_16x16x32_bf16 v[72:75], v[164:167], a[148:151], v[72:75]\n"
    "v_exp_f32 v148, v148\n"
    "v_subrev_f32 v219, s75, v219\n"
    "v_mfma_f32_16x16x32_bf16 v[76:79], v[160:163], a[152:155], v[76:79]\n"
    "ds_read_b128 a[96:99], v201 offset:40960\n"
    "v_mul_f32 v212, s24, v212\n"
    "v_mfma_f32_16x16x32_bf16 v[76:79], v[164:167], a[156:159], v[76:79]\n"
    "v_exp_f32 v149, v149\n"
    "v_mfma_f32_16x16x32_bf16 v[80:83], v[160:163], a[160:163], v[80:83]\n"
    "ds_read_b128 a[100:103], v201 offset:41984\n"
    "v_cvt_pk_bf16_f32 v170, v148, v149\n"
    "v_mfma_f32_16x16x32_bf16 v[80:83], v[164:167], a[164:167], v[80:83]\n"
    "v_exp_f32 v150, v150\n"
    "v_mfma_f32_16x16x32_bf16 v[84:87], v[160:163], a[168:171], v[84:87]\n"
    "ds_read_b128 a[104:107], v201 offset:43008\n"
    "v_mul_f32 v213, s25, v213\n"
    "v_mfma_f32_16x16x32_bf16 v[84:87], v[164:167], a[172:175], v[84:87]\n"
    "v_exp_f32 v151, v151\n"
    "v_mfma_f32_16x16x32_bf16 v[88:91], v[160:163], a[176:179], v[88:91]\n"
    "ds_read_b128 a[108:111], v201 offset:44032\n"
    "v_cvt_pk_bf16_f32 v171, v150, v151\n"
    "v_mfma_f32_16x16x32_bf16 v[88:91], v[164:167], a[180:183], v[88:91]\n"
    "v_exp_f32 v152, v152\n"
    "v_mul_f32 v214, s26, v214\n"
    "v_mfma_f32_16x16x32_bf16 v[92:95], v[160:163], a[184:187], v[92:95]\n"
    "ds_read_b128 a[124:127], v201 offset:48128\n"
    "v_mul_f32 v215, s27, v215\n"
    "v_mfma_f32_16x16x32_bf16 v[92:95], v[164:167], a[188:191], v[92:95]\n"
    "v_exp_f32 v153, v153\n"
    "v_smfmac_f32_16x16x64_bf16 a[204:207], a[192:195], v[160:167], v240\n"
    "s_waitcnt lgkmcnt(4)\n"
    "ds_read_b128 a[120:123], v201 offset:47104\n"
    "v_mfma_f32_16x16x32_bf16 v[128:131], a[64:67], a[0:3], v[176:179]\n"
    "v_exp_f32 v154, v154\n"
    "v_mfma_f32_16x16x32_bf16 v[128:131], a[68:71], a[4:7], v[128:131]\n"
    "ds_read_b128 a[116:119], v201 offset:46080\n"
    "v_cvt_pk_bf16_f32 v172, v152, v153\n"
    "v_mul_f32 v216, s28, v216\n"
    "v_mfma_f32_16x16x32_bf16 v[128:131], a[72:75], a[8:11], v[128:131]\n"
    "v_exp_f32 v155, v155\n"
    "v_mfma_f32_16x16x32_bf16 v[128:131], a[76:79], a[12:15], v[128:131]\n"
    "ds_read_b128 a[112:115], v201 offset:45056\n"
    "v_cvt_pk_bf16_f32 v173, v154, v155\n"
    "v_mul_f32 v217, s29, v217\n"
    "v_mfma_f32_16x16x32_bf16 v[132:135], a[92:95], a[12:15], v[176:179]\n"
    "v_exp_f32 v156, v156\n"
    "v_mfma_f32_16x16x32_bf16 v[132:135], a[88:91], a[8:11], v[132:135]\n"
    "v_exp_f32 v157, v157\n"
    "v_mfma_f32_16x16x32_bf16 v[132:135], a[84:87], a[4:7], v[132:135]\n"
    "v_exp_f32 v158, v158\n"
    "v_mul_f32 v218, s30, v218\n"
    "v_mfma_f32_16x16x32_bf16 v[132:135], a[80:83], a[0:3], v[132:135]\n"
    "v_exp_f32 v159, v159\n"
    "v_mfma_f32_16x16x32_bf16 v[136:139], a[96:99], a[0:3], v[176:179]\n"
    "s_waitcnt lgkmcnt(2)\n"
    "v_cvt_pk_bf16_f32 v174, v156, v157\n"
    "v_cvt_pk_bf16_f32 v175, v158, v159\n"
    "v_mul_f32 v219, s31, v219\n"
    "v_mfma_f32_16x16x32_bf16 v[136:139], a[100:103], a[4:7], v[136:139]\n"
    "s_add_u32 m0, s35, 0x2000\n"
    "v_cvt_pk_bf16_f32 v212, v212, v213\n"
    "v_cvt_pk_bf16_f32 v213, v214, v215\n"
    "v_mfma_f32_16x16x32_bf16 v[136:139], a[104:107], a[8:11], v[136:139]\n"
    "global_load_lds_dwordx4 v242, s[56:57] offset:2048\n"
    "v_mfma_f32_16x16x32_bf16 v[136:139], a[108:111], a[12:15], v[136:139]\n"
    "global_load_lds_dwordx4 v242, s[84:85] offset:3072\n"
    "v_cvt_pk_bf16_f32 v214, v216, v217\n"
    "v_mfma_f32_16x16x32_bf16 v[140:143], a[124:127], a[12:15], v[176:179]\n"
    "v_exp_f32 v128, v128\n"
    "v_mfma_f32_16x16x32_bf16 v[140:143], a[120:123], a[8:11], v[140:143]\n"
    "s_waitcnt lgkmcnt(0)\n"
    "v_exp_f32 v129, v129\n"
    "v_mfma_f32_16x16x32_bf16 v[140:143], a[116:119], a[4:7], v[140:143]\n"
    "v_exp_f32 v130, v130\n"
    "v_mfma_f32_16x16x32_bf16 v[140:143], a[112:115], a[0:3], v[140:143]\n"
    "v_cvt_pk_bf16_f32 v160, v128, v129\n"
    "v_cvt_pk_bf16_f32 v215, v218, v219\n"
    "v_mfma_f32_16x16x32_bf16 v[96:99], v[168:171], a[128:131], v[96:99]\n"
    "global_store_dwordx4 v244, v[212:215], s[44:45] sc1\n"
    "v_add_u32 v244, 0x40000, v244\n"
    "v_mfma_f32_16x16x32_bf16 v[96:99], v[172:175], a[132:135], v[96:99]\n"
    "202: s_cbranch_execz 199b\n"
    "v_exp_f32 v131, v131\n"
    "v_mfma_f32_16x16x32_bf16 v[100:103], v[168:171], a[136:139], v[100:103]\n"
    "ds_read_b128 a[128:131], v201 offset:49152\n"
    "v_cvt_pk_bf16_f32 v161, v130, v131\n"
    "v_mfma_f32_16x16x32_bf16 v[100:103], v[172:175], a[140:143], v[100:103]\n"
    "v_exp_f32 v132, v132\n"
    "v_mfma_f32_16x16x32_bf16 v[104:107], v[168:171], a[144:147], v[104:107]\n"
    "ds_read_b128 a[132:135], v201 offset:57344\n"
    "v_mfma_f32_16x16x32_bf16 v[104:107], v[172:175], a[148:151], v[104:107]\n"
    "v_exp_f32 v133, v133\n"
    "v_mfma_f32_16x16x32_bf16 v[108:111], v[168:171], a[152:155], v[108:111]\n"
    "ds_read_b128 a[136:139], v201 offset:50176\n"
    "v_cvt_pk_bf16_f32 v162, v132, v133\n"
    "v_mfma_f32_16x16x32_bf16 v[108:111], v[172:175], a[156:159], v[108:111]\n"
    "v_exp_f32 v134, v134\n"
    "v_mfma_f32_16x16x32_bf16 v[112:115], v[168:171], a[160:163], v[112:115]\n"
    "v_exp_f32 v135, v135\n"
    "v_mfma_f32_16x16x32_bf16 v[112:115], v[172:175], a[164:167], v[112:115]\n"
    "ds_read_b128 a[140:143], v201 offset:58368\n"
    "v_cvt_pk_bf16_f32 v163, v134, v135\n"
    "v_mfma_f32_16x16x32_bf16 v[116:119], v[168:171], a[168:171], v[116:119]\n"
    "v_exp_f32 v136, v136\n"
    "v_mfma_f32_16x16x32_bf16 v[116:119], v[172:175], a[172:175], v[116:119]\n"
    "ds_read_b128 a[144:147], v201 offset:51200\n"
    "v_mfma_f32_16x16x32_bf16 v[120:123], v[168:171], a[176:179], v[120:123]\n"
    "v_exp_f32 v137, v137\n"
    "v_mfma_f32_16x16x32_bf16 v[120:123], v[172:175], a[180:183], v[120:123]\n"
    "ds_read_b128 a[148:151], v201 offset:59392\n"
    "v_cvt_pk_bf16_f32 v164, v136, v137\n"
    "v_mfma_f32_16x16x32_bf16 v[124:127], v[168:171], a[184:187], v[124:127]\n"
    "v_exp_f32 v138, v138\n"
    "v_mfma_f32_16x16x32_bf16 v[124:127], v[172:175], a[188:191], v[124:127]\n"
    "ds_read_b128 a[152:155], v201 offset:52224\n"
    "v_smfmac_f32_16x16x64_bf16 a[208:211], a[192:195], v[168:175], v240\n"
    "ds_read_b128 a[156:159], v201 offset:60416\n"
    "v_mfma_f32_16x16x32_bf16 v[144:147], a[64:67], a[16:19], v[180:183]\n"
    "v_exp_f32 v139, v139\n"
    "v_mfma_f32_16x16x32_bf16 v[144:147], a[68:71], a[20:23], v[144:147]\n"
    "v_exp_f32 v140, v140\n"
    "v_mfma_f32_16x16x32_bf16 v[144:147], a[72:75], a[24:27], v[144:147]\n"
    "ds_read_b128 a[160:163], v201 offset:53248\n"
    "v_cvt_pk_bf16_f32 v165, v138, v139\n"
    "v_mfma_f32_16x16x32_bf16 v[144:147], a[76:79], a[28:31], v[144:147]\n"
    "v_exp_f32 v141, v141\n"
    "v_mfma_f32_16x16x32_bf16 v[148:151], a[92:95], a[28:31], v[180:183]\n"
    "ds_read_b128 a[164:167], v201 offset:61440\n"
    "v_cvt_pk_bf16_f32 v166, v140, v141\n"
    "v_mfma_f32_16x16x32_bf16 v[148:151], a[88:91], a[24:27], v[148:151]\n"
    "v_exp_f32 v142, v142\n"
    "v_mfma_f32_16x16x32_bf16 v[148:151], a[84:87], a[20:23], v[148:151]\n"
    "ds_read_b128 a[168:171], v201 offset:54272\n"
    "v_mfma_f32_16x16x32_bf16 v[148:151], a[80:83], a[16:19], v[148:151]\n"
    "global_load_dwordx2 v[252:253], v245, s[66:67] offset:256 sc1\n"
    "v_mfma_f32_16x16x32_bf16 v[152:155], a[96:99], a[16:19], v[180:183]\n"
    "v_exp_f32 v143, v143\n"
    "v_mfma_f32_16x16x32_bf16 v[152:155], a[100:103], a[20:23], v[152:155]\n"
    "ds_read_b128 a[172:175], v201 offset:62464\n"
    "v_cvt_pk_bf16_f32 v167, v142, v143\n"
    "v_mfma_f32_16x16x32_bf16 v[152:155], a[104:107], a[24:27], v[152:155]\n"
    "v_exp_f32 v144, v144\n"
    "v_mfma_f32_16x16x32_bf16 v[152:155], a[108:111], a[28:31], v[152:155]\n"
    "ds_read_b128 a[176:179], v201 offset:55296\n"
    "v_mfma_f32_16x16x32_bf16 v[156:159], a[124:127], a[28:31], v[180:183]\n"
    "v_exp_f32 v145, v145\n"
    "v_mfma_f32_16x16x32_bf16 v[156:159], a[120:123], a[24:27], v[156:159]\n"
    "ds_read_b128 a[180:183], v201 offset:63488\n"
    "v_cvt_pk_bf16_f32 v168, v144, v145\n"
    "v_mfma_f32_16x16x32_bf16 v[156:159], a[116:119], a[20:23], v[156:159]\n"
    "v_exp_f32 v146, v146\n"
    "v_mfma_f32_16x16x32_bf16 v[156:159], a[112:115], a[16:19], v[156:159]\n"
    "s_waitcnt lgkmcnt(3)\n"
    "ds_read_b128 a[184:187], v201 offset:56320\n"
    "v_mfma_f32_16x16x32_bf16 v[0:3], v[160:163], a[128:131], v[0:3]\n"
    "v_exp_f32 v147, v147\n"
    "v_mfma_f32_16x16x32_bf16 v[0:3], v[164:167], a[132:135], v[0:3]\n"
    "ds_read_b128 a[188:191], v201 offset:64512\n"
    "v_cvt_pk_bf16_f32 v169, v146, v147\n"
    "v_mfma_f32_16x16x32_bf16 v[4:7], v[160:163], a[136:139], v[4:7]\n"
    "v_exp_f32 v148, v148\n"
    "v_mfma_f32_16x16x32_bf16 v[4:7], v[164:167], a[140:143], v[4:7]\n"
    "v_exp_f32 v149, v149\n"
    "v_mfma_f32_16x16x32_bf16 v[8:11], v[160:163], a[144:147], v[8:11]\n"
    "v_cvt_pk_bf16_f32 v170, v148, v149\n"
    "v_mfma_f32_16x16x32_bf16 v[8:11], v[164:167], a[148:151], v[8:11]\n"
    "v_exp_f32 v150, v150\n"
    "v_mfma_f32_16x16x32_bf16 v[12:15], v[160:163], a[152:155], v[12:15]\n"
    "v_exp_f32 v151, v151\n"
    "v_mfma_f32_16x16x32_bf16 v[12:15], v[164:167], a[156:159], v[12:15]\n"
    "v_cvt_pk_bf16_f32 v171, v150, v151\n"
    "v_mfma_f32_16x16x32_bf16 v[16:19], v[160:163], a[160:163], v[16:19]\n"
    "v_exp_f32 v152, v152\n"
    "v_mfma_f32_16x16x32_bf16 v[16:19], v[164:167], a[164:167], v[16:19]\n"
    "v_exp_f32 v153, v153\n"
    "v_mfma_f32_16x16x32_bf16 v[20:23], v[160:163], a[168:171], v[20:23]\n"
    "s_waitcnt lgkmcnt(0)\n"
    "v_cvt_pk_bf16_f32 v172, v152, v153\n"
    "v_mfma_f32_16x16x32_bf16 v[20:23], v[164:167], a[172:175], v[20:23]\n"
    "v_exp_f32 v154, v154\n"
    "v_mfma_f32_16x16x32_bf16 v[24:27], v[160:163], a[176:179], v[24:27]\n"
    "v_exp_f32 v155, v155\n"
    "v_mfma_f32_16x16x32_bf16 v[24:27], v[164:167], a[180:183], v[24:27]\n"
    "v_cvt_pk_bf16_f32 v173, v154, v155\n"
    "v_mfma_f32_16x16x32_bf16 v[28:31], v[160:163], a[184:187], v[28:31]\n"
    "v_exp_f32 v156, v156\n"
    "v_mfma_f32_16x16x32_bf16 v[28:31], v[164:167], a[188:191], v[28:31]\n"
    "v_exp_f32 v157, v157\n"
    "v_smfmac_f32_16x16x64_bf16 a[196:199], a[192:195], v[160:167], v240\n"
    "s_add_u32 m0, s6, 0x8000\n"
    "v_cvt_pk_bf16_f32 v174, v156, v157\n"
    "v_mfma_f32_16x16x32_bf16 v[128:131], a[64:67], a[32:35], v[184:187]\n"
    "v_exp_f32 v158, v158\n"
    "v_mfma_f32_16x16x32_bf16 v[128:131], a[68:71], a[36:39], v[128:131]\n"
    "v_exp_f32 v159, v159\n"
    "v_mfma_f32_16x16x32_bf16 v[128:131], a[72:75], a[40:43], v[128:131]\n"
    "s_barrier\n"
    "v_mfma_f32_16x16x32_bf16 v[128:131], a[76:79], a[44:47], v[128:131]\n"
    "global_load_lds_dwordx4 v201, s[40:41] offset:0 sc1\n"
    "v_cvt_pk_bf16_f32 v175, v158, v159\n"
    "v_mfma_f32_16x16x32_bf16 v[132:135], a[92:95], a[44:47], v[184:187]\n"
    "v_mfma_f32_16x16x32_bf16 v[132:135], a[88:91], a[40:43], v[132:135]\n"
    "v_mfma_f32_16x16x32_bf16 v[132:135], a[84:87], a[36:39], v[132:135]\n"
    "v_mfma_f32_16x16x32_bf16 v[132:135], a[80:83], a[32:35], v[132:135]\n"
    "v_mfma_f32_16x16x32_bf16 v[136:139], a[96:99], a[32:35], v[184:187]\n"
    "v_mfma_f32_16x16x32_bf16 v[136:139], a[100:103], a[36:39], v[136:139]\n"
    "v_mfma_f32_16x16x32_bf16 v[136:139], a[104:107], a[40:43], v[136:139]\n"
    "global_load_lds_dwordx4 v201, s[40:41] offset:1024 sc1\n"
    "v_mfma_f32_16x16x32_bf16 v[136:139], a[108:111], a[44:47], v[136:139]\n"
    "v_exp_f32 v128, v128\n"
    "v_mfma_f32_16x16x32_bf16 v[140:143], a[124:127], a[44:47], v[184:187]\n"
    "v_exp_f32 v129, v129\n"
    "v_mfma_f32_16x16x32_bf16 v[140:143], a[120:123], a[40:43], v[140:143]\n"
    "v_cvt_pk_bf16_f32 v160, v128, v129\n"
    "v_mfma_f32_16x16x32_bf16 v[140:143], a[116:119], a[36:39], v[140:143]\n"
    "v_exp_f32 v130, v130\n"
    "v_mfma_f32_16x16x32_bf16 v[140:143], a[112:115], a[32:35], v[140:143]\n"
    "v_exp_f32 v131, v131\n"
    "v_mfma_f32_16x16x32_bf16 v[32:35], v[168:171], a[128:131], v[32:35]\n"
    "v_cvt_pk_bf16_f32 v161, v130, v131\n"
    "v_mfma_f32_16x16x32_bf16 v[32:35], v[172:175], a[132:135], v[32:35]\n"
    "global_load_lds_dwordx4 v201, s[40:41] offset:2048 sc1\n"
    "v_mfma_f32_16x16x32_bf16 v[36:39], v[168:171], a[136:139], v[36:39]\n"
    "global_load_lds_dwordx4 v201, s[40:41] offset:3072 sc1\n"
    "v_mfma_f32_16x16x32_bf16 v[36:39], v[172:175], a[140:143], v[36:39]\n"
    "s_add_u32 m0, s6, 0x9000\n"
    "v_exp_f32 v132, v132\n"
    "v_mfma_f32_16x16x32_bf16 v[40:43], v[168:171], a[144:147], v[40:43]\n"
    "v_exp_f32 v133, v133\n"
    "v_mfma_f32_16x16x32_bf16 v[40:43], v[172:175], a[148:151], v[40:43]\n"
    "v_cvt_pk_bf16_f32 v162, v132, v133\n"
    "v_mfma_f32_16x16x32_bf16 v[44:47], v[168:171], a[152:155], v[44:47]\n"
    "v_exp_f32 v134, v134\n"
    "v_mfma_f32_16x16x32_bf16 v[44:47], v[172:175], a[156:159], v[44:47]\n"
    "v_exp_f32 v135, v135\n"
    "v_mfma_f32_16x16x32_bf16 v[48:51], v[168:171], a[160:163], v[48:51]\n"
    "v_cvt_pk_bf16_f32 v163, v134, v135\n"
    "v_mfma_f32_16x16x32_bf16 v[48:51], v[172:175], a[164:167], v[48:51]\n"
    "global_load_lds_dwordx4 v202, s[40:41] offset:0 sc1\n"
    "v_mfma_f32_16x16x32_bf16 v[52:55], v[168:171], a[168:171], v[52:55]\n"
    "v_exp_f32 v136, v136\n"
    "v_mfma_f32_16x16x32_bf16 v[52:55], v[172:175], a[172:175], v[52:55]\n"
    "v_exp_f32 v137, v137\n"
    "v_mfma_f32_16x16x32_bf16 v[56:59], v[168:171], a[176:179], v[56:59]\n"
    "v_cvt_pk_bf16_f32 v164, v136, v137\n"
    "v_mfma_f32_16x16x32_bf16 v[56:59], v[172:175], a[180:183], v[56:59]\n"
    "v_exp_f32 v138, v138\n"
    "v_mfma_f32_16x16x32_bf16 v[60:63], v[168:171], a[184:187], v[60:63]\n"
    "s_waitcnt vmcnt(9)\n"
    "ds_read_b128 v[204:207], v249 offset:8192\n"
    "v_mfma_f32_16x16x32_bf16 v[60:63], v[172:175], a[188:191], v[60:63]\n"
    "ds_read_b128 v[208:211], v249 offset:8208\n"
    "v_smfmac_f32_16x16x64_bf16 a[200:203], a[192:195], v[168:175], v240\n"
    "s_waitcnt vmcnt(7)\n"
    "ds_read2st64_b32 v[212:213], v251 offset0:32 offset1:33\n"
    "v_mfma_f32_16x16x32_bf16 v[144:147], a[64:67], a[48:51], v[188:191]\n"
    "global_load_lds_dwordx4 v202, s[40:41] offset:1024 sc1\n"
    "v_mfma_f32_16x16x32_bf16 v[144:147], a[68:71], a[52:55], v[144:147]\n"
    "ds_read2st64_b32 v[214:215], v251 offset0:34 offset1:35\n"
    "v_mfma_f32_16x16x32_bf16 v[144:147], a[72:75], a[56:59], v[144:147]\n"
    "v_exp_f32 v139, v139\n"
    "v_mfma_f32_16x16x32_bf16 v[144:147], a[76:79], a[60:63], v[144:147]\n"
    "ds_read_b128 a[64:67], v203 offset:0\n"
    "ds_read2st64_b32 v[216:217], v251 offset0:36 offset1:37\n"
    "v_mfma_f32_16x16x32_bf16 v[148:151], a[92:95], a[60:63], v[188:191]\n"
    "ds_read2st64_b32 v[218:219], v251 offset0:38 offset1:39\n"
    "v_cvt_pk_bf16_f32 v165, v138, v139\n"
    "v_mfma_f32_16x16x32_bf16 v[148:151], a[88:91], a[56:59], v[148:151]\n"
    "v_exp_f32 v140, v140\n"
    "v_mfma_f32_16x16x32_bf16 v[148:151], a[84:87], a[52:55], v[148:151]\n"
    "v_exp_f32 v141, v141\n"
    "v_mfma_f32_16x16x32_bf16 v[148:151], a[80:83], a[48:51], v[148:151]\n"
    "global_load_lds_dwordx4 v202, s[40:41] offset:2048 sc1\n"
    "v_mfma_f32_16x16x32_bf16 v[152:155], a[96:99], a[48:51], v[188:191]\n"
    "v_exp_f32 v142, v142\n"
    "v_mfma_f32_16x16x32_bf16 v[152:155], a[100:103], a[52:55], v[152:155]\n"
    "ds_read_b128 a[68:71], v203 offset:1024\n"
    "v_cvt_pk_bf16_f32 v166, v140, v141\n"
    "v_mfma_f32_16x16x32_bf16 v[152:155], a[104:107], a[56:59], v[152:155]\n"
    "s_waitcnt lgkmcnt(4)\n"
    "v_exp_f32 v143, v143\n"
    "v_cvt_f32_i32 v204, v204\n"
    "v_mfma_f32_16x16x32_bf16 v[152:155], a[108:111], a[60:63], v[152:155]\n"
    "ds_read_b128 a[72:75], v203 offset:2048\n"
    "v_cvt_pk_bf16_f32 v167, v142, v143\n"
    "v_mfma_f32_16x16x32_bf16 v[156:159], a[124:127], a[60:63], v[188:191]\n"
    "ds_read_b128 a[76:79], v203 offset:3072\n"
    "v_cvt_f32_i32 v205, v205\n"
    "v_mfma_f32_16x16x32_bf16 v[156:159], a[120:123], a[56:59], v[156:159]\n"
    "v_exp_f32 v144, v144\n"
    "v_mfma_f32_16x16x32_bf16 v[156:159], a[116:119], a[52:55], v[156:159]\n"
    "global_load_lds_dwordx4 v202, s[40:41] offset:3072 sc1\n"
    "v_mfma_f32_16x16x32_bf16 v[156:159], a[112:115], a[48:51], v[156:159]\n"
    "s_add_u32 s40, s40, 0x8000\n"
    "v_exp_f32 v145, v145\n"
    "v_cvt_f32_i32 v206, v206\n"
    "v_mfma_f32_16x16x32_bf16 v[64:67], v[160:163], a[128:131], v[64:67]\n"
    "s_addc_u32 s41, s41, 0\n"
    "ds_read_b128 a[92:95], v203 offset:7168\n"
    "v_cvt_pk_bf16_f32 v168, v144, v145\n"
    "v_mfma_f32_16x16x32_bf16 v[64:67], v[164:167], a[132:135], v[64:67]\n"
    "s_waitcnt vmcnt(8)\n"
    "v_exp_f32 v146, v146\n"
    "v_cvt_f32_i32 v207, v207\n"
    "v_mfma_f32_16x16x32_bf16 v[68:71], v[160:163], a[136:139], v[68:71]\n"
    "v_cmp_ne_u64 vcc, v[252:253], v[246:247]\n"
    "s_cbranch_vccz 28f\n"
    "s_mov_b32 s48, 20000\n"
    "27:\n"
    "global_load_dwordx2 v[252:253], v245, s[66:67] offset:256 sc1\n"
    " s_waitcnt vmcnt(0)\n"
    "v_cmp_ne_u64 vcc, v[252:253], v[246:247]\n"
    "s_cbranch_vccz 28f\n"
    "s_sleep 1\n"
    "s_sub_u32 s48, s48, 1\n"
    "s_cbranch_scc0 27b\n"
    "s_mov_b32 s34, 1\n"
    "28:\n"
    "v_xor_b32 v246, 1, v246\n"
    "ds_read_b128 a[88:91], v203 offset:6144\n"
    "v_cvt_f32_i32 v208, v208\n"
    "v_mfma_f32_16x16x32_bf16 v[68:71], v[164:167], a[140:143], v[68:71]\n"
    "v_exp_f32 v147, v147\n"
    "v_mfma_f32_16x16x32_bf16 v[72:75], v[160:163], a[144:147], v[72:75]\n"
    "ds_read_b128 a[84:87], v203 offset:5120\n"
    "v_cvt_pk_bf16_f32 v169, v146, v147\n"
    "v_mfma_f32_16x16x32_bf16 v[72:75], v[164:167], a[148:151], v[72:75]\n"
    "v_exp_f32 v148, v148\n"
    "v_mfma_f32_16x16x32_bf16 v[76:79], v[160:163], a[152:155], v[76:79]\n"
    "ds_read_b128 a[80:83], v203 offset:4096\n"
    "v_cvt_f32_i32 v209, v209\n"
    "v_mfma_f32_16x16x32_bf16 v[76:79], v[164:167], a[156:159], v[76:79]\n"
    "v_exp_f32 v149, v149\n"
    "v_mfma_f32_16x16x32_bf16 v[80:83], v[160:163], a[160:163], v[80:83]\n"
    "ds_read_b128 a[96:99], v203 offset:8192\n"
    "v_cvt_pk_bf16_f32 v170, v148, v149\n"
    "v_mfma_f32_16x16x32_bf16 v[80:83], v[164:167], a[164:167], v[80:83]\n"
    "v_exp_f32 v150, v150\n"
    "v_cvt_f32_i32 v210, v210\n"
    "v_mfma_f32_16x16x32_bf16 v[84:87], v[160:163], a[168:171], v[84:87]\n"
    "ds_read_b128 a[100:103], v203 offset:9216\n"
    "v_cvt_f32_i32 v211, v211\n"
    "v_mfma_f32_16x16x32_bf16 v[84:87], v[164:167], a[172:175], v[84:87]\n"
    "v_exp_f32 v151, v151\n"
    "v_mfma_f32_16x16x32_bf16 v[88:91], v[160:163], a[176:179], v[88:91]\n"
    "ds_read_b128 a[104:107], v203 offset:10240\n"
    "v_cvt_pk_bf16_f32 v171, v150, v151\n"
    "v_mfma_f32_16x16x32_bf16 v[88:91], v[164:167], a[180:183], v[88:91]\n"
    "v_exp_f32 v152, v152\n"
    "v_mfma_f32_16x16x32_bf16 v[92:95], v[160:163], a[184:187], v[92:95]\n"
    "ds_read_b128 a[108:111], v203 offset:11264\n"
    "v_fma_f32 v204, v204, v220, v228\n"
    "v_mfma_f32_16x16x32_bf16 v[92:95], v[164:167], a[188:191], v[92:95]\n"
    "v_exp_f32 v153, v153\n"
    "v_smfmac_f32_16x16x64_bf16 a[204:207], a[192:195], v[160:167], v240\n"
    "s_waitcnt lgkmcnt(4)\n"
    "ds_read_b128 a[124:127], v203 offset:15360\n"
    "v_fma_f32 v205, v205, v221, v229\n"
    "v_mfma_f32_16x16x32_bf16 v[128:131], a[64:67], a[0:3], v[176:179]\n"
    "v_exp_f32 v154, v154\n"
    "v_mfma_f32_16x16x32_bf16 v[128:131], a[68:71], a[4:7], v[128:131]\n"
    "ds_read_b128 a[120:123], v203 offset:14336\n"
    "v_cvt_pk_bf16_f32 v172, v152, v153\n"
    "v_mfma_f32_16x16x32_bf16 v[128:131], a[72:75], a[8:11], v[128:131]\n"
    "v_exp_f32 v155, v155\n"
    "v_fma_f32 v206, v206, v222, v230\n"
    "v_mfma_f32_16x16x32_bf16 v[128:131], a[76:79], a[12:15], v[128:131]\n"
    "ds_read_b128 a[116:119], v203 offset:13312\n"
    "v_cvt_pk_bf16_f32 v173, v154, v155\n"
    "v_mfma_f32_16x16x32_bf16 v[132:135], a[92:95], a[12:15], v[176:179]\n"
    "v_exp_f32 v156, v156\n"
    "v_fma_f32 v207, v207, v223, v231\n"
    "v_mfma_f32_16x16x32_bf16 v[132:135], a[88:91], a[8:11], v[132:135]\n"
    "ds_read_b128 a[112:115], v203 offset:12288\n"
    "v_fma_f32 v208, v208, v224, v232\n"
    "v_mfma_f32_16x16x32_bf16 v[132:135], a[84:87], a[4:7], v[132:135]\n"
    "v_exp_f32 v157, v157\n"
    "v_mfma_f32_16x16x32_bf16 v[132:135], a[80:83], a[0:3], v[132:135]\n"
    "s_waitcnt lgkmcnt(4)\n"
    "v_exp_f32 v158, v158\n"
    "v_mfma_f32_16x16x32_bf16 v[136:139], a[96:99], a[0:3], v[176:179]\n"
    "v_exp_f32 v159, v159\n"
    "v_mfma_f32_16x16x32_bf16 v[136:139], a[100:103], a[4:7], v[136:139]\n"
    "v_cvt_pk_bf16_f32 v174, v156, v157\n"
    "v_cvt_pk_bf16_f32 v175, v158, v159\n"
    "v_fma_f32 v209, v209, v225, v233\n"
    "v_mfma_f32_16x16x32_bf16 v[136:139], a[104:107], a[8:11], v[136:139]\n"
    "v_exp_f32 v128, v128\n"
    "v_mfma_f32_16x16x32_bf16 v[136:139], a[108:111], a[12:15], v[136:139]\n"
    "s_waitcnt lgkmcnt(2)\n"
    "v_fma_f32 v210, v210, v226, v234\n"
    "v_fma_f32 v211, v211, v227, v235\n"
    "v_mfma_f32_16x16x32_bf16 v[140:143], a[124:127], a[12:15], v[176:179]\n"
    "v_exp_f32 v129, v129\n"
    "v_mfma_f32_16x16x32_bf16 v[140:143], a[120:123], a[8:11], v[140:143]\n"
    "s_waitcnt lgkmcnt(1)\n"
    "v_cvt_pk_bf16_f32 v160, v128, v129\n"
    "v_cvt_pk_bf16_f32 v204, v204, v205\n"
    "v_mfma_f32_16x16x32_bf16 v[140:143], a[116:119], a[4:7], v[140:143]\n"
    "s_waitcnt lgkmcnt(0)\n"
    "v_exp_f32 v130, v130\n"
    "v_mfma_f32_16x16x32_bf16 v[140:143], a[112:115], a[0:3], v[140:143]\n"
    "v_cvt_pk_bf16_f32 v205, v206, v207\n"
    "v_cvt_pk_bf16_f32 v206, v208, v209\n"
    "v_mfma_f32_16x16x32_bf16 v[96:99], v[168:171], a[128:131], v[96:99]\n"
    "v_exp_f32 v131, v131\n"
    "v_mfma_f32_16x16x32_bf16 v[96:99], v[172:175], a[132:135], v[96:99]\n"
    "ds_read_b128 a[128:131], v203 offset:16384\n"
    "v_cvt_pk_bf16_f32 v161, v130, v131\n"
    "v_mfma_f32_16x16x32_bf16 v[100:103], v[168:171], a[136:139], v[100:103]\n"
    "v_exp_f32 v132, v132\n"
    "v_mfma_f32_16x16x32_bf16 v[100:103], v[172:175], a[140:143], v[100:103]\n"
    "ds_read_b128 a[132:135], v203 offset:24576\n"
    "v_cvt_pk_bf16_f32 v207, v210, v211\n"
    "v_mfma_f32_16x16x32_bf16 v[104:107], v[168:171], a[144:147], v[104:107]\n"
    "global_store_dwordx4 v243, v[204:207], s[44:45] sc1\n"
    "v_cvt_f32_i32 v212, v212\n"
    "v_mfma_f32_16x16x32_bf16 v[104:107], v[172:175], a[148:151], v[104:107]\n"
    "v_exp_f32 v133, v133\n"
    "v_mfma_f32_16x16x32_bf16 v[108:111], v[168:171], a[152:155], v[108:111]\n"
    "ds_read_b128 a[136:139], v203 offset:17408\n"
    "v_cvt_pk_bf16_f32 v162, v132, v133\n"
    "v_mfma_f32_16x16x32_bf16 v[108:111], v[172:175], a[156:159], v[108:111]\n"
    "v_exp_f32 v134, v134\n"
    "v_mfma_f32_16x16x32_bf16 v[112:115], v[168:171], a[160:163], v[112:115]\n"
    "v_exp_f32 v135, v135\n"
    "v_mfma_f32_16x16x32_bf16 v[112:115], v[172:175], a[164:167], v[112:115]\n"
    "ds_read_b128 a[140:143], v203 offset:25600\n"
    "v_cvt_pk_bf16_f32 v163, v134, v135\n"
    "v_mfma_f32_16x16x32_bf16 v[116:119], v[168:171], a[168:171], v[116:119]\n"
    "v_exp_f32 v136, v136\n"
    "v_mfma_f32_16x16x32_bf16 v[116:119], v[172:175], a[172:175], v[116:119]\n"
    "ds_read_b128 a[144:147], v203 offset:18432\n"
    "v_cvt_f32_i32 v213, v213\n"
    "v_mfma_f32_16x16x32_bf16 v[120:123], v[168:171], a[176:179], v[120:123]\n"
    "v_exp_f32 v137, v137\n"
    "v_mfma_f32_16x16x32_bf16 v[120:123], v[172:175], a[180:183], v[120:123]\n"
    "ds_read_b128 a[148:151], v203 offset:26624\n"
    "v_cvt_pk_bf16_f32 v164, v136, v137\n"
    "v_mfma_f32_16x16x32_bf16 v[124:127], v[168:171], a[184:187], v[124:127]\n"
    "v_exp_f32 v138, v138\n"
    "v_mfma_f32_16x16x32_bf16 v[124:127], v[172:175], a[188:191], v[124:127]\n"
    "ds_read_b128 a[152:155], v203 offset:19456\n"
    "v_cvt_f32_i32 v214, v214\n"
    "v_smfmac_f32_16x16x64_bf16 a[208:211], a[192:195], v[168:175], v240\n"
    "ds_read_b128 a[156:159], v203 offset:27648\n"
    "v_mfma_f32_16x16x32_bf16 v[144:147], a[64:67], a[16:19], v[180:183]\n"
    "v_exp_f32 v139, v139\n"
    "v_mfma_f32_16x16x32_bf16 v[144:147], a[68:71], a[20:23], v[144:147]\n"
    "v_exp_f32 v140, v140\n"
    "v_cvt_f32_i32 v215, v215\n"
    "v_mfma_f32_16x16x32_bf16 v[144:147], a[72:75], a[24:27], v[144:147]\n"
    "ds_read_b128 a[160:163], v203 offset:20480\n"
    "v_cvt_pk_bf16_f32 v165, v138, v139\n"
    "v_mfma_f32_16x16x32_bf16 v[144:147], a[76:79], a[28:31], v[144:147]\n"
    "v_exp_f32 v141, v141\n"
    "v_mfma_f32_16x16x32_bf16 v[148:151], a[92:95], a[28:31], v[180:183]\n"
    "ds_read_b128 a[164:167], v203 offset:28672\n"
    "v_cvt_pk_bf16_f32 v166, v140, v141\n"
    "v_cvt_f32_i32 v216, v216\n"
    "v_mfma_f32_16x16x32_bf16 v[148:151], a[88:91], a[24:27], v[148:151]\n"
    "v_exp_f32 v142, v142\n"
    "v_mfma_f32_16x16x32_bf16 v[148:151], a[84:87], a[20:23], v[148:151]\n"
    "ds_read_b128 a[168:171], v203 offset:21504\n"
    "v_cvt_f32_i32 v217, v217\n"
    "v_mfma_f32_16x16x32_bf16 v[148:151], a[80:83], a[16:19], v[148:151]\n"
    "v_exp_f32 v143, v143\n"
    "v_mfma_f32_16x16x32_bf16 v[152:155], a[96:99], a[16:19], v[180:183]\n"
    "ds_read_b128 a[172:175], v203 offset:29696\n"
    "v_cvt_pk_bf16_f32 v167, v142, v143\n"
    "v_mfma_f32_16x16x32_bf16 v[152:155], a[100:103], a[20:23], v[152:155]\n"
    "v_cvt_f32_i32 v218, v218\n"
    "v_cvt_f32_i32 v219, v219\n"
    "v_mfma_f32_16x16x32_bf16 v[152:155], a[104:107], a[24:27], v[152:155]\n"
    "v_exp_f32 v144, v144\n"
    "v_mfma_f32_16x16x32_bf16 v[152:155], a[108:111], a[28:31], v[152:155]\n"
    "ds_read_b128 a[176:179], v203 offset:22528\n"
    "v_subrev_f32 v212, s88, v212\n"
    "v_mfma_f32_16x16x32_bf16 v[156:159], a[124:127], a[28:31], v[180:183]\n"
    "v_exp_f32 v145, v145\n"
    "v_mfma_f32_16x16x32_bf16 v[156:159], a[120:123], a[24:27], v[156:159]\n"
    "ds_read_b128 a[180:183], v203 offset:30720\n"
    "v_cvt_pk_bf16_f32 v168, v144, v145\n"
    "v_mfma_f32_16x16x32_bf16 v[156:159], a[116:119], a[20:23], v[156:159]\n"
    "v_exp_f32 v146, v146\n"
    "v_mfma_f32_16x16x32_bf16 v[156:159], a[112:115], a[16:19], v[156:159]\n"
    "s_waitcnt lgkmcnt(3)\n"
    "ds_read_b128 a[184:187], v203 offset:23552\n"
    "v_subrev_f32 v213, s89, v213\n"
    "v_mfma_f32_16x16x32_bf16 v[0:3], v[160:163], a[128:131], v[0:3]\n"
    "v_exp_f32 v147, v147\n"
    "v_mfma_f32_16x16x32_bf16 v[0:3], v[164:167], a[132:135], v[0:3]\n"
    "ds_read_b128 a[188:191], v203 offset:31744\n"
    "v_cvt_pk_bf16_f32 v169, v146, v147\n"
    "v_mfma_f32_16x16x32_bf16 v[4:7], v[160:163], a[136:139], v[4:7]\n"
    "v_exp_f32 v148, v148\n"
    "v_mfma_f32_16x16x32_bf16 v[4:7], v[164:167], a[140:143], v[4:7]\n"
    "v_subrev_f32 v214, s90, v214\n"
    "v_subrev_f32 v215, s91, v215\n"
    "v_mfma_f32_16x16x32_bf16 v[8:11], v[160:163], a[144:147], v[8:11]\n"
    "v_exp_f32 v149, v149\n"
    "v_mfma_f32_16x16x32_bf16 v[8:11], v[164:167], a[148:151], v[8:11]\n"
    "v_cvt_pk_bf16_f32 v170, v148, v149\n"
    "v_subrev_f32 v216, s92, v216\n"
    "v_mfma_f32_16x16x32_bf16 v[12:15], v[160:163], a[152:155], v[12:15]\n"
    "v_exp_f32 v150, v150\n"
    "v_mfma_f32_16x16x32_bf16 v[12:15], v[164:167], a[156:159], v[12:15]\n"
    "v_subrev_f32 v217, s93, v217\n"
    "v_subrev_f32 v218, s94, v218\n"
    "v_mfma_f32_16x16x32_bf16 v[16:19], v[160:163], a[160:163], v[16:19]\n"
    "v_exp_f32 v151, v151\n"
    "v_mfma_f32_16x16x32_bf16 v[16:19], v[164:167], a[164:167], v[16:19]\n"
    "v_cvt_pk_bf16_f32 v171, v150, v151\n"
    "v_subrev_f32 v219, s95, v219\n"
    "v_mfma_f32_16x16x32_bf16 v[20:23], v[160:163], a[168:171], v[20:23]\n"
    "s_waitcnt lgkmcnt(0)\n"
    "v_exp_f32 v152, v152\n"
    "v_mfma_f32_16x16x32_bf16 v[20:23], v[164:167], a[172:175], v[20:23]\n"
    "v_mul_f32 v212, s76, v212\n"
    "v_mul_f32 v213, s77, v213\n"
    "v_mfma_f32_16x16x32_bf16 v[24:27], v[160:163], a[176:179], v[24:27]\n"
    "v_exp_f32 v153, v153\n"
    "v_mfma_f32_16x16x32_bf16 v[24:27], v[164:167], a[180:183], v[24:27]\n"
    "v_cvt_pk_bf16_f32 v172, v152, v153\n"
    "v_mul_f32 v214, s78, v214\n"
    "v_mfma_f32_16x16x32_bf16 v[28:31], v[160:163], a[184:187], v[28:31]\n"
    "v_exp_f32 v154, v154\n"
    "v_mfma_f32_16x16x32_bf16 v[28:31], v[164:167], a[188:191], v[28:31]\n"
    "v_mul_f32 v215, s79, v215\n"
    "v_mul_f32 v216, s80, v216\n"
    "v_smfmac_f32_16x16x64_bf16 a[196:199], a[192:195], v[160:167], v240\n"
    "s_add_u32 m0, s6, 0x10000\n"
    "v_mul_f32 v217, s81, v217\n"
    "v_mfma_f32_16x16x32_bf16 v[128:131], a[64:67], a[32:35], v[184:187]\n"
    "v_exp_f32 v155, v155\n"
    "v_mfma_f32_16x16x32_bf16 v[128:131], a[68:71], a[36:39], v[128:131]\n"
    "v_exp_f32 v156, v156\n"
    "v_mfma_f32_16x16x32_bf16 v[128:131], a[72:75], a[40:43], v[128:131]\n"
    "s_barrier\n"
    "v_mfma_f32_16x16x32_bf16 v[128:131], a[76:79], a[44:47], v[128:131]\n"
    "global_load_lds_dwordx4 v201, s[40:41] offset:0 sc1\n"
    "v_cvt_pk_bf16_f32 v173, v154, v155\n"
    "v_mfma_f32_16x16x32_bf16 v[132:135], a[92:95], a[44:47], v[184:187]\n"
    "v_exp_f32 v157, v157\n"
    "v_mfma_f32_16x16x32_bf16 v[132:135], a[88:91], a[40:43], v[132:135]\n"
    "v_exp_f32 v158, v158\n"
    "v_mfma_f32_16x16x32_bf16 v[132:135], a[84:87], a[36:39], v[132:135]\n"
    "v_cvt_pk_bf16_f32 v174, v156, v157\n"
    "v_mul_f32 v218, s82, v218\n"
    "v_mfma_f32_16x16x32_bf16 v[132:135], a[80:83], a[32:35], v[132:135]\n"
    "v_exp_f32 v159, v159\n"
    "v_mfma_f32_16x16x32_bf16 v[136:139], a[96:99], a[32:35], v[184:187]\n"
    "v_cvt_pk_bf16_f32 v175, v158, v159\n"
    "v_mul_f32 v219, s83, v219\n"
    "v_mfma_f32_16x16x32_bf16 v[136:139], a[100:103], a[36:39], v[136:139]\n"
    "v_cvt_pk_bf16_f32 v212, v212, v213\n"
    "v_cvt_pk_bf16_f32 v213, v214, v215\n"
    "v_mfma_f32_16x16x32_bf16 v[136:139], a[104:107], a[40:43], v[136:139]\n"
    "global_load_lds_dwordx4 v201, s[40:41] offset:1024 sc1\n"
    "v_mfma_f32_16x16x32_bf16 v[136:139], a[108:111], a[44:47], v[136:139]\n"
    "v_exp_f32 v128, v128\n"
    "v_mfma_f32_16x16x32_bf16 v[140:143], a[124:127], a[44:47], v[184:187]\n"
    "v_exp_f32 v129, v129\n"
    "v_mfma_f32_16x16x32_bf16 v[140:143], a[120:123], a[40:43], v[140:143]\n"
    "v_cvt_pk_bf16_f32 v160, v128, v129\n"
    "v_cvt_pk_bf16_f32 v214, v216, v217\n"
    "v_mfma_f32_16x16x32_bf16 v[140:143], a[116:119], a[36:39], v[140:143]\n"
    "v_exp_f32 v130, v130\n"
    "v_mfma_f32_16x16x32_bf16 v[140:143], a[112:115], a[32:35], v[140:143]\n"
    "v_cvt_pk_bf16_f32 v215, v218, v219\n"
    "v_mfma_f32_16x16x32_bf16 v[32:35], v[168:171], a[128:131], v[32:35]\n"
    "global_store_dwordx4 v244, v[212:215], s[44:45] sc1\n"
    "v_mfma_f32_16x16x32_bf16 v[32:35], v[172:175], a[132:135], v[32:35]\n"
    "v_exp_f32 v131, v131\n"
    "v_mfma_f32_16x16x32_bf16 v[36:39], v[168:171], a[136:139], v[36:39]\n"
    "v_exp_f32 v132, v132\n"
    "v_mfma_f32_16x16x32_bf16 v[36:39], v[172:175], a[140:143], v[36:39]\n"
    "global_load_lds_dwordx4 v201, s[40:41] offset:2048 sc1\n"
    "v_mfma_f32_16x16x32_bf16 v[40:43], v[168:171], a[144:147], v[40:43]\n"
    "global_load_lds_dwordx4 v201, s[40:41] offset:3072 sc1\n"
    "v_cvt_pk_bf16_f32 v161, v130, v131\n"
    "v_mfma_f32_16x16x32_bf16 v[40:43], v[172:175], a[148:151], v[40:43]\n"
    "s_add_u32 m0, s6, 0x11000\n"
    "v_exp_f32 v133, v133\n"
    "v_mfma_f32_16x16x32_bf16 v[44:47], v[168:171], a[152:155], v[44:47]\n"
    "v_exp_f32 v134, v134\n"
    "v_mfma_f32_16x16x32_bf16 v[44:47], v[172:175], a[156:159], v[44:47]\n"
    "v_cvt_pk_bf16_f32 v162, v132, v133\n"
    "v_mfma_f32_16x16x32_bf16 v[48:51], v[168:171], a[160:163], v[48:51]\n"
    "v_exp_f32 v135, v135\n"
    "v_mfma_f32_16x16x32_bf16 v[48:51], v[172:175], a[164:167], v[48:51]\n"
    "v_cvt_pk_bf16_f32 v163, v134, v135\n"
    "v_mfma_f32_16x16x32_bf16 v[52:55], v[168:171], a[168:171], v[52:55]\n"
    "v_exp_f32 v136, v136\n"
    "v_mfma_f32_16x16x32_bf16 v[52:55], v[172:175], a[172:175], v[52:55]\n"
    "global_load_lds_dwordx4 v202, s[40:41] offset:0 sc1\n"
    "v_mfma_f32_16x16x32_bf16 v[56:59], v[168:171], a[176:179], v[56:59]\n"
    "global_load_lds_dwordx4 v202, s[40:41] offset:1024 sc1\n"
    "v_mfma_f32_16x16x32_bf16 v[56:59], v[172:175], a[180:183], v[56:59]\n"
    "v_exp_f32 v137, v137\n"
    "v_mfma_f32_16x16x32_bf16 v[60:63], v[168:171], a[184:187], v[60:63]\n"
    "v_exp_f32 v138, v138\n"
    "v_mfma_f32_16x16x32_bf16 v[60:63], v[172:175], a[188:191], v[60:63]\n"
    "v_cvt_pk_bf16_f32 v164, v136, v137\n"
    "v_smfmac_f32_16x16x64_bf16 a[200:203], a[192:195], v[168:175], v240\n"
    "v_mfma_f32_16x16x32_bf16 v[144:147], a[64:67], a[48:51], v[188:191]\n"
    "v_exp_f32 v139, v139\n"
    "v_mfma_f32_16x16x32_bf16 v[144:147], a[68:71], a[52:55], v[144:147]\n"
    "v_exp_f32 v140, v140\n"
    "v_mfma_f32_16x16x32_bf16 v[144:147], a[72:75], a[56:59], v[144:147]\n"
    "ds_read_b128 a[64:67], v201 offset:0\n"
    "v_cvt_pk_bf16_f32 v165, v138, v139\n"
    "v_mfma_f32_16x16x32_bf16 v[144:147], a[76:79], a[60:63], v[144:147]\n"
    "global_load_lds_dwordx4 v202, s[40:41] offset:2048 sc1\n"
    "v_mfma_f32_16x16x32_bf16 v[148:151], a[92:95], a[60:63], v[188:191]\n"
    "ds_read_b128 a[68:71], v201 offset:1024\n"
    "v_mfma_f32_16x16x32_bf16 v[148:151], a[88:91], a[56:59], v[148:151]\n"
    "v_exp_f32 v141, v141\n"
    "v_mfma_f32_16x16x32_bf16 v[148:151], a[84:87], a[52:55], v[148:151]\n"
    "ds_read_b128 a[72:75], v201 offset:2048\n"
    "v_cvt_pk_bf16_f32 v166, v140, v141\n"
    "v_mfma_f32_16x16x32_bf16 v[148:151], a[80:83], a[48:51], v[148:151]\n"
    "v_exp_f32 v142, v142\n"
    "v_mfma_f32_16x16x32_bf16 v[152:155], a[96:99], a[48:51], v[188:191]\n"
    "v_exp_f32 v143, v143\n"
    "v_mfma_f32_16x16x32_bf16 v[152:155], a[100:103], a[52:55], v[152:155]\n"
    "ds_read_b128 a[76:79], v201 offset:3072\n"
    "v_cvt_pk_bf16_f32 v167, v142, v143\n"
    "v_mfma_f32_16x16x32_bf16 v[152:155], a[104:107], a[56:59], v[152:155]\n"
    "v_exp_f32 v144, v144\n"
    "v_mfma_f32_16x16x32_bf16 v[152:155], a[108:111], a[60:63], v[152:155]\n"
    "global_load_lds_dwordx4 v202, s[40:41] offset:3072 sc1\n"
    "v_mfma_f32_16x16x32_bf16 v[156:159], a[124:127], a[60:63], v[188:191]\n"
    "s_add_u32 s40, s40, 0x8000\n"
    "ds_read_b128 a[92:95], v201 offset:7168\n"
    "v_mfma_f32_16x16x32_bf16 v[156:159], a[120:123], a[56:59], v[156:159]\n"
    "s_addc_u32 s41, s41, 0\n"
    "ds_read_b128 a[88:91], v201 offset:6144\n"
    "v_mfma_f32_16x16x32_bf16 v[156:159], a[116:119], a[52:55], v[156:159]\n"
    "v_exp_f32 v145, v145\n"
    "v_mfma_f32_16x16x32_bf16 v[156:159], a[112:115], a[48:51], v[156:159]\n"
    "ds_read_b128 a[84:87], v201 offset:5120\n"
    "v_cvt_pk_bf16_f32 v168, v144, v145\n"
    "v_mfma_f32_16x16x32_bf16 v[64:67], v[160:163], a[128:131], v[64:67]\n"
    "v_exp_f32 v146, v146\n"
    "v_mfma_f32_16x16x32_bf16 v[64:67], v[164:167], a[132:135], v[64:67]\n"
    "ds_read_b128 a[80:83], v201 offset:4096\n"
    "v_mfma_f32_16x16x32_bf16 v[68:71], v[160:163], a[136:139], v[68:71]\n"
    "v_exp_f32 v147, v147\n"
    "v_mfma_f32_16x16x32_bf16 v[68:71], v[164:167], a[140:143], v[68:71]\n"
    "v_exp_f32 v148, v148\n"
    "v_mfma_f32_16x16x32_bf16 v[72:75], v[160:163], a[144:147], v[72:75]\n"
    "ds_read_b128 a[96:99], v201 offset:8192\n"
    "v_cvt_pk_bf16_f32 v169, v146, v147\n"
    "v_mfma_f32_16x16x32_bf16 v[72:75], v[164:167], a[148:151], v[72:75]\n"
    "v_exp_f32 v149, v149\n"
    "v_mfma_f32_16x16x32_bf16 v[76:79], v[160:163], a[152:155], v[76:79]\n"
    "ds_read_b128 a[100:103], v201 offset:9216\n"
    "v_cvt_pk_bf16_f32 v170, v148, v149\n"
    "v_mfma_f32_16x16x32_bf16 v[76:79], v[164:167], a[156:159], v[76:79]\n"
    "v_exp_f32 v150, v150\n"
    "v_mfma_f32_16x16x32_bf16 v[80:83], v[160:163], a[160:163], v[80:83]\n"
    "ds_read_b128 a[104:107], v201 offset:10240\n"
    "v_mfma_f32_16x16x32_bf16 v[80:83], v[164:167], a[164:167], v[80:83]\n"
    "v_exp_f32 v151, v151\n"
    "v_mfma_f32_16x16x32_bf16 v[84:87], v[160:163], a[168:171], v[84:87]\n"
    "ds_read_b128 a[108:111], v201 offset:11264\n"
    "v_cvt_pk_bf16_f32 v171, v150, v151\n"
    "v_mfma_f32_16x16x32_bf16 v[84:87], v[164:167], a[172:175], v[84:87]\n"
    "v_exp_f32 v152, v152\n"
    "v_mfma_f32_16x16x32_bf16 v[88:91], v[160:163], a[176:179], v[88:91]\n"
    "ds_read_b128 a[124:127], v201 offset:15360\n"
    "v_mfma_f32_16x16x32_bf16 v[88:91], v[164:167], a[180:183], v[88:91]\n"
    "v_exp_f32 v153, v153\n"
    "v_mfma_f32_16x16x32_bf16 v[92:95], v[160:163], a[184:187], v[92:95]\n"
    "v_exp_f32 v154, v154\n"
    "v_mfma_f32_16x16x32_bf16 v[92:95], v[164:167], a[188:191], v[92:95]\n"
    "ds_read_b128 a[120:123], v201 offset:14336\n"
    "v_cvt_pk_bf16_f32 v172, v152, v153\n"
    "v_smfmac_f32_16x16x64_bf16 a[204:207], a[192:195], v[160:167], v240\n"
    "s_waitcnt lgkmcnt(4)\n"
    "ds_read_b128 a[116:119], v201 offset:13312\n"
    "v_mfma_f32_16x16x32_bf16 v[128:131], a[64:67], a[0:3], v[176:179]\n"
    "v_exp_f32 v155, v155\n"
    "v_mfma_f32_16x16x32_bf16 v[128:131], a[68:71], a[4:7], v[128:131]\n"
    "203: s_cbranch_execz 204f\n"
    "v_exp_f32 v156, v156\n"
    "v_mfma_f32_16x16x32_bf16 v[128:131], a[72:75], a[8:11], v[128:131]\n"
    "ds_read_b128 a[112:115], v201 offset:12288\n"
    "v_cvt_pk_bf16_f32 v173, v154, v155\n"
    "v_mfma_f32_16x16x32_bf16 v[128:131], a[76:79], a[12:15], v[128:131]\n"
    "v_exp_f32 v157, v157\n"
    "v_mfma_f32_16x16x32_bf16 v[132:135], a[92:95], a[12:15], v[176:179]\n"
    "v_cvt_pk_bf16_f32 v174, v156, v157\n"
    "v_mfma_f32_16x16x32_bf16 v[132:135], a[88:91], a[8:11], v[132:135]\n"
    "v_exp_f32 v158, v158\n"
    "v_mfma_f32_16x16x32_bf16 v[132:135], a[84:87], a[4:7], v[132:135]\n"
    "v_exp_f32 v159, v159\n"
    "v_mfma_f32_16x16x32_bf16 v[132:135], a[80:83], a[0:3], v[132:135]\n"
    "v_cvt_pk_bf16_f32 v175, v158, v159\n"
    "v_mfma_f32_16x16x32_bf16 v[136:139], a[96:99], a[0:3], v[176:179]\n"
    "v_mfma_f32_16x16x32_bf16 v[136:139], a[100:103], a[4:7], v[136:139]\n"
    "s_waitcnt lgkmcnt(1)\n"
    "v_mfma_f32_16x16x32_bf16 v[136:139], a[104:107], a[8:11], v[136:139]\n"
    "v_exp_f32 v128, v128\n"
    "v_mfma_f32_16x16x32_bf16 v[136:139], a[108:111], a[12:15], v[136:139]\n"
    "v_exp_f32 v129, v129\n"
    "v_mfma_f32_16x16x32_bf16 v[140:143], a[124:127], a[12:15], v[176:179]\n"
    "v_cvt_pk_bf16_f32 v160, v128, v129\n"
    "v_mfma_f32_16x16x32_bf16 v[140:143], a[120:123], a[8:11], v[140:143]\n"
    "v_exp_f32 v130, v130\n"
    "v_mfma_f32_16x16x32_bf16 v[140:143], a[116:119], a[4:7], v[140:143]\n"
    "s_waitcnt lgkmcnt(0)\n"
    "v_exp_f32 v131, v131\n"
    "v_mfma_f32_16x16x32_bf16 v[140:143], a[112:115], a[0:3], v[140:143]\n"
    "v_cvt_pk_bf16_f32 v161, v130, v131\n"
    "v_mfma_f32_16x16x32_bf16 v[96:99], v[168:171], a[128:131], v[96:99]\n"
    "v_exp_f32 v132, v132\n"
    "v_mfma_f32_16x16x32_bf16 v[96:99], v[172:175], a[132:135], v[96:99]\n"
    "ds_read_b128 a[128:131], v201 offset:16384\n"
    "v_mfma_f32_16x16x32_bf16 v[100:103], v[168:171], a[136:139], v[100:103]\n"
    "v_exp_f32 v133, v133\n"
    "v_mfma_f32_16x16x32_bf16 v[100:103], v[172:175], a[140:143], v[100:103]\n"
    "ds_read_b128 a[132:135], v201 offset:24576\n"
    "v_cvt_pk_bf16_f32 v162, v132, v133\n"
    "v_mfma_f32_16x16x32_bf16 v[104:107], v[168:171], a[144:147], v[104:107]\n"
    "v_exp_f32 v134, v134\n"
    "v_mfma_f32_16x16x32_bf16 v[104:107], v[172:175], a[148:151], v[104:107]\n"
    "ds_read_b128 a[136:139], v201 offset:17408\n"
    "v_mfma_f32_16x16x32_bf16 v[108:111], v[168:171], a[152:155], v[108:111]\n"
    "v_exp_f32 v135, v135\n"
    "v_mfma_f32_16x16x32_bf16 v[108:111], v[172:175], a[156:159], v[108:111]\n"
    "v_cvt_pk_bf16_f32 v163, v134, v135\n"
    "v_mfma_f32_16x16x32_bf16 v[112:115], v[168:171], a[160:163], v[112:115]\n"
    "ds_read_b128 a[140:143], v201 offset:25600\n"
    "v_mfma_f32_16x16x32_bf16 v[112:115], v[172:175], a[164:167], v[112:115]\n"
    "v_exp_f32 v136, v136\n"
    "v_mfma_f32_16x16x32_bf16 v[116:119], v[168:171], a[168:171], v[116:119]\n"
    "ds_read_b128 a[144:147], v201 offset:18432\n"
    "v_mfma_f32_16x16x32_bf16 v[116:119], v[172:175], a[172:175], v[116:119]\n"
    "v_exp_f32 v137, v137\n"
    "v_mfma_f32_16x16x32_bf16 v[120:123], v[168:171], a[176:179], v[120:123]\n"
    "ds_read_b128 a[148:151], v201 offset:26624\n"
    "v_cvt_pk_bf16_f32 v164, v136, v137\n"
    "v_mfma_f32_16x16x32_bf16 v[120:123], v[172:175], a[180:183], v[120:123]\n"
    "v_exp_f32 v138, v138\n"
    "v_mfma_f32_16x16x32_bf16 v[124:127], v[168:171], a[184:187], v[124:127]\n"
    "ds_read_b128 a[152:155], v201 offset:19456\n"
    "v_mfma_f32_16x16x32_bf16 v[124:127], v[172:175], a[188:191], v[124:127]\n"
    "v_exp_f32 v139, v139\n"
    "v_smfmac_f32_16x16x64_bf16 a[208:211], a[192:195], v[168:175], v240\n"
    "ds_read_b128 a[156:159], v201 offset:27648\n"
    "v_mfma_f32_16x16x32_bf16 v[144:147], a[64:67], a[16:19], v[180:183]\n"
    "v_cvt_pk_bf16_f32 v165, v138, v139\n"
    "v_mfma_f32_16x16x32_bf16 v[144:147], a[68:71], a[20:23], v[144:147]\n"
    "v_exp_f32 v140, v140\n"
    "v_mfma_f32_16x16x32_bf16 v[144:147], a[72:75], a[24:27], v[144:147]\n"
    "ds_read_b128 a[160:163], v201 offset:20480\n"
    "v_mfma_f32_16x16x32_bf16 v[144:147], a[76:79], a[28:31], v[144:147]\n"
    "v_exp_f32 v141, v141\n"
    "v_mfma_f32_16x16x32_bf16 v[148:151], a[92:95], a[28:31], v[180:183]\n"
    "ds_read_b128 a[164:167], v201 offset:28672\n"
    "v_cvt_pk_bf16_f32 v166, v140, v141\n"
    "v_mfma_f32_16x16x32_bf16 v[148:151], a[88:91], a[24:27], v[148:151]\n"
    "v_exp_f32 v142, v142\n"
    "v_mfma_f32_16x16x32_bf16 v[148:151], a[84:87], a[20:23], v[148:151]\n"
    "ds_read_b128 a[168:171], v201 offset:21504\n"
    "v_mfma_f32_16x16x32_bf16 v[148:151], a[80:83], a[16:19], v[148:151]\n"
    "v_exp_f32 v143, v143\n"
    "v_mfma_f32_16x16x32_bf16 v[152:155], a[96:99], a[16:19], v[180:183]\n"
    "ds_read_b128 a[172:175], v201 offset:29696\n"
    "v_cvt_pk_bf16_f32 v167, v142, v143\n"
    "v_mfma_f32_16x16x32_bf16 v[152:155], a[100:103], a[20:23], v[152:155]\n"
    "v_mfma_f32_16x16x32_bf16 v[152:155], a[104:107], a[24:27], v[152:155]\n"
    "v_exp_f32 v144, v144\n"
    "v_mfma_f32_16x16x32_bf16 v[152:155], a[108:111], a[28:31], v[152:155]\n"
    "ds_read_b128 a[176:179], v201 offset:22528\n"
    "v_mfma_f32_16x16x32_bf16 v[156:159], a[124:127], a[28:31], v[180:183]\n"
    "v_exp_f32 v145, v145\n"
    "v_mfma_f32_16x16x32_bf16 v[156:159], a[120:123], a[24:27], v[156:159]\n"
    "ds_read_b128 a[180:183], v201 offset:30720\n"
    "v_cvt_pk_bf16_f32 v168, v144, v145\n"
    "v_mfma_f32_16x16x32_bf16 v[156:159], a[116:119], a[20:23], v[156:159]\n"
    "v_exp_f32 v146, v146\n"
    "v_mfma_f32_16x16x32_bf16 v[156:159], a[112:115], a[16:19], v[156:159]\n"
    "s_waitcnt lgkmcnt(3)\n"
    "ds_read_b128 a[184:187], v201 offset:23552\n"
    "v_mfma_f32_16x16x32_bf16 v[0:3], v[160:163], a[128:131], v[0:3]\n"
    "v_exp_f32 v147, v147\n"
    "v_mfma_f32_16x16x32_bf16 v[0:3], v[164:167], a[132:135], v[0:3]\n"
    "ds_read_b128 a[188:191], v201 offset:31744\n"
    "v_cvt_pk_bf16_f32 v169, v146, v147\n"
    "v_mfma_f32_16x16x32_bf16 v[4:7], v[160:163], a[136:139], v[4:7]\n"
    "v_exp_f32 v148, v148\n"
    "v_mfma_f32_16x16x32_bf16 v[4:7], v[164:167], a[140:143], v[4:7]\n"
    "v_exp_f32 v149, v149\n"
    "v_mfma_f32_16x16x32_bf16 v[8:11], v[160:163], a[144:147], v[8:11]\n"
    "v_cvt_pk_bf16_f32 v170, v148, v149\n"
    "v_mfma_f32_16x16x32_bf16 v[8:11], v[164:167], a[148:151], v[8:11]\n"
    "v_exp_f32 v150, v150\n"
    "v_mfma_f32_16x16x32_bf16 v[12:15], v[160:163], a[152:155], v[12:15]\n"
    "v_exp_f32 v151, v151\n"
    "v_mfma_f32_16x16x32_bf16 v[12:15], v[164:167], a[156:159], v[12:15]\n"
    "v_cvt_pk_bf16_f32 v171, v150, v151\n"
    "v_mfma_f32_16x16x32_bf16 v[16:19], v[160:163], a[160:163], v[16:19]\n"
    "v_exp_f32 v152, v152\n"
    "v_mfma_f32_16x16x32_bf16 v[16:19], v[164:167], a[164:167], v[16:19]\n"
    "v_exp_f32 v153, v153\n"
    "v_mfma_f32_16x16x32_bf16 v[20:23], v[160:163], a[168:171], v[20:23]\n"
    "s_waitcnt lgkmcnt(0)\n"
    "v_cvt_pk_bf16_f32 v172, v152, v153\n"
    "v_mfma_f32_16x16x32_bf16 v[20:23], v[164:167], a[172:175], v[20:23]\n"
    "v_exp_f32 v154, v154\n"
    "v_mfma_f32_16x16x32_bf16 v[24:27], v[160:163], a[176:179], v[24:27]\n"
    "v_exp_f32 v155, v155\n"
    "v_mfma_f32_16x16x32_bf16 v[24:27], v[164:167], a[180:183], v[24:27]\n"
    "v_cvt_pk_bf16_f32 v173, v154, v155\n"
    "v_mfma_f32_16x16x32_bf16 v[28:31], v[160:163], a[184:187], v[28:31]\n"
    "v_exp_f32 v156, v156\n"
    "v_mfma_f32_16x16x32_bf16 v[28:31], v[164:167], a[188:191], v[28:31]\n"
    "v_exp_f32 v157, v157\n"
    "v_smfmac_f32_16x16x64_bf16 a[196:199], a[192:195], v[160:167], v240\n"
    "s_add_u32 m0, s6, 0x0\n"
    "v_cvt_pk_bf16_f32 v174, v156, v157\n"
    "v_mfma_f32_16x16x32_bf16 v[128:131], a[64:67], a[32:35], v[184:187]\n"
    "v_exp_f32 v158, v158\n"
    "v_mfma_f32_16x16x32_bf16 v[128:131], a[68:71], a[36:39], v[128:131]\n"
    "s_waitcnt vmcnt(0)\n"
    "v_exp_f32 v159, v159\n"
    "v_mfma_f32_16x16x32_bf16 v[128:131], a[72:75], a[40:43], v[128:131]\n"
    "s_barrier\n"
    "s_cmp_lg_u32 %[w], 0\n"
    "s_cbranch_scc1 29f\n"
    "global_store_dwordx2 v248, v[246:247], s[66:67] offset:0 sc1\n"
    "29:\n"
    "v_mfma_f32_16x16x32_bf16 v[128:131], a[76:79], a[44:47], v[128:131]\n"
    "global_load_lds_dwordx4 v201, s[40:41] offset:0 sc1\n"
    "v_cvt_pk_bf16_f32 v175, v158, v159\n"
    "v_mfma_f32_16x16x32_bf16 v[132:135], a[92:95], a[44:47], v[184:187]\n"
    "v_mfma_f32_16x16x32_bf16 v[132:135], a[88:91], a[40:43], v[132:135]\n"
    "v_mfma_f32_16x16x32_bf16 v[132:135], a[84:87], a[36:39], v[132:135]\n"
    "v_mfma_f32_16x16x32_bf16 v[132:135], a[80:83], a[32:35], v[132:135]\n"
    "v_mfma_f32_16x16x32_bf16 v[136:139], a[96:99], a[32:35], v[184:187]\n"
    "v_mfma_f32_16x16x32_bf16 v[136:139], a[100:103], a[36:39], v[136:139]\n"
    "v_mfma_f32_16x16x32_bf16 v[136:139], a[104:107], a[40:43], v[136:139]\n"
    "global_load_lds_dwordx4 v201, s[40:41] offset:1024 sc1\n"
    "v_mfma_f32_16x16x32_bf16 v[136:139], a[108:111], a[44:47], v[136:139]\n"
    "v_exp_f32 v128, v128\n"
    "v_mfma_f32_16x16x32_bf16 v[140:143], a[124:127], a[44:47], v[184:187]\n"
    "v_exp_f32 v129, v129\n"
    "v_mfma_f32_16x16x32_bf16 v[140:143], a[120:123], a[40:43], v[140:143]\n"
    "v_cvt_pk_bf16_f32 v160, v128, v129\n"
    "v_mfma_f32_16x16x32_bf16 v[140:143], a[116:119], a[36:39], v[140:143]\n"
    "v_exp_f32 v130, v130\n"
    "v_mfma_f32_16x16x32_bf16 v[140:143], a[112:115], a[32:35], v[140:143]\n"
    "v_exp_f32 v131, v131\n"
    "v_mfma_f32_16x16x32_bf16 v[32:35], v[168:171], a[128:131], v[32:35]\n"
    "v_cvt_pk_bf16_f32 v161, v130, v131\n"
    "v_mfma_f32_16x16x32_bf16 v[32:35], v[172:175], a[132:135], v[32:35]\n"
    "global_load_lds_dwordx4 v201, s[40:41] offset:2048 sc1\n"
    "v_mfma_f32_16x16x32_bf16 v[36:39], v[168:171], a[136:139], v[36:39]\n"
    "global_load_lds_dwordx4 v201, s[40:41] offset:3072 sc1\n"
    "v_mfma_f32_16x16x32_bf16 v[36:39], v[172:175], a[140:143], v[36:39]\n"
    "s_add_u32 m0, s6, 0x1000\n"
    "v_exp_f32 v132, v132\n"
    "v_mfma_f32_16x16x32_bf16 v[40:43], v[168:171], a[144:147], v[40:43]\n"
    "v_exp_f32 v133, v133\n"
    "v_mfma_f32_16x16x32_bf16 v[40:43], v[172:175], a[148:151], v[40:43]\n"
    "v_cvt_pk_bf16_f32 v162, v132, v133\n"
    "v_mfma_f32_16x16x32_bf16 v[44:47], v[168:171], a[152:155], v[44:47]\n"
    "v_exp_f32 v134, v134\n"
    "v_mfma_f32_16x16x32_bf16 v[44:47], v[172:175], a[156:159], v[44:47]\n"
    "v_exp_f32 v135, v135\n"
    "v_mfma_f32_16x16x32_bf16 v[48:51], v[168:171], a[160:163], v[48:51]\n"
    "v_cvt_pk_bf16_f32 v163, v134, v135\n"
    "v_mfma_f32_16x16x32_bf16 v[48:51], v[172:175], a[164:167], v[48:51]\n"
    "global_load_lds_dwordx4 v202, s[40:41] offset:0 sc1\n"
    "v_mfma_f32_16x16x32_bf16 v[52:55], v[168:171], a[168:171], v[52:55]\n"
    "v_exp_f32 v136, v136\n"
    "v_mfma_f32_16x16x32_bf16 v[52:55], v[172:175], a[172:175], v[52:55]\n"
    "v_exp_f32 v137, v137\n"
    "v_mfma_f32_16x16x32_bf16 v[56:59], v[168:171], a[176:179], v[56:59]\n"
    "v_cvt_pk_bf16_f32 v164, v136, v137\n"
    "v_mfma_f32_16x16x32_bf16 v[56:59], v[172:175], a[180:183], v[56:59]\n"
    "v_exp_f32 v138, v138\n"
    "v_mfma_f32_16x16x32_bf16 v[60:63], v[168:171], a[184:187], v[60:63]\n"
    "v_exp_f32 v139, v139\n"
    "v_mfma_f32_16x16x32_bf16 v[60:63], v[172:175], a[188:191], v[60:63]\n"
    "v_cvt_pk_bf16_f32 v165, v138, v139\n"
    "v_smfmac_f32_16x16x64_bf16 a[200:203], a[192:195], v[168:175], v240\n"
    "v_mfma_f32_16x16x32_bf16 v[144:147], a[64:67], a[48:51], v[188:191]\n"
    "global_load_lds_dwordx4 v202, s[40:41] offset:1024 sc1\n"
    "v_mfma_f32_16x16x32_bf16 v[144:147], a[68:71], a[52:55], v[144:147]\n"
    "v_exp_f32 v140, v140\n"
    "v_mfma_f32_16x16x32_bf16 v[144:147], a[72:75], a[56:59], v[144:147]\n"
    "ds_read_b128 a[64:67], v201 offset:32768\n"
    "v_mfma_f32_16x16x32_bf16 v[144:147], a[76:79], a[60:63], v[144:147]\n"
    "v_exp_f32 v141, v141\n"
    "v_mfma_f32_16x16x32_bf16 v[148:151], a[92:95], a[60:63], v[188:191]\n"
    "ds_read_b128 a[68:71], v201 offset:33792\n"
    "v_cvt_pk_bf16_f32 v166, v140, v141\n"
    "v_mfma_f32_16x16x32_bf16 v[148:151], a[88:91], a[56:59], v[148:151]\n"
    "v_exp_f32 v142, v142\n"
    "v_mfma_f32_16x16x32_bf16 v[148:151], a[84:87], a[52:55], v[148:151]\n"
    "ds_read_b128 a[72:75], v201 offset:34816\n"
    "v_mfma_f32_16x16x32_bf16 v[148:151], a[80:83], a[48:51], v[148:151]\n"
    "global_load_lds_dwordx4 v202, s[40:41] offset:2048 sc1\n"
    "v_mfma_f32_16x16x32_bf16 v[152:155], a[96:99], a[48:51], v[188:191]\n"
    "v_exp_f32 v143, v143\n"
    "v_mfma_f32_16x16x32_bf16 v[152:155], a[100:103], a[52:55], v[152:155]\n"
    "ds_read_b128 a[76:79], v201 offset:35840\n"
    "v_cvt_pk_bf16_f32 v167, v142, v143\n"
    "v_mfma_f32_16x16x32_bf16 v[152:155], a[104:107], a[56:59], v[152:155]\n"
    "v_exp_f32 v144, v144\n"
    "v_mfma_f32_16x16x32_bf16 v[152:155], a[108:111], a[60:63], v[152:155]\n"
    "ds_read_b128 a[92:95], v201 offset:39936\n"
    "v_mfma_f32_16x16x32_bf16 v[156:159], a[124:127], a[60:63], v[188:191]\n"
    "v_exp_f32 v145, v145\n"
    "v_mfma_f32_16x16x32_bf16 v[156:159], a[120:123], a[56:59], v[156:159]\n"
    "ds_read_b128 a[88:91], v201 offset:38912\n"
    "v_cvt_pk_bf16_f32 v168, v144, v145\n"
    "v_mfma_f32_16x16x32_bf16 v[156:159], a[116:119], a[52:55], v[156:159]\n"
    "global_load_lds_dwordx4 v202, s[40:41] offset:3072 sc1\n"
    "v_mfma_f32_16x16x32_bf16 v[156:159], a[112:115], a[48:51], v[156:159]\n"
    "s_add_u32 s40, s40, 0x8000\n"
    "ds_read_b128 a[84:87], v201 offset:37888\n"
    "v_mfma_f32_16x16x32_bf16 v[64:67], v[160:163], a[128:131], v[64:67]\n"
    "s_addc_u32 s41, s41, 0\n"
    "v_exp_f32 v146, v146\n"
    "v_mfma_f32_16x16x32_bf16 v[64:67], v[164:167], a[132:135], v[64:67]\n"
    "ds_read_b128 a[80:83], v201 offset:36864\n"
    "v_mfma_f32_16x16x32_bf16 v[68:71], v[160:163], a[136:139], v[68:71]\n"
    "v_exp_f32 v147, v147\n"
    "v_mfma_f32_16x16x32_bf16 v[68:71], v[164:167], a[140:143], v[68:71]\n"
    "v_exp_f32 v148, v148\n"
    "v_mfma_f32_16x16x32_bf16 v[72:75], v[160:163], a[144:147], v[72:75]\n"
    "ds_read_b128 a[96:99], v201 offset:40960\n"
    "v_cvt_pk_bf16_f32 v169, v146, v147\n"
    "v_mfma_f32_16x16x32_bf16 v[72:75], v[164:167], a[148:151], v[72:75]\n"
    "v_exp_f32 v149, v149\n"
    "v_mfma_f32_16x16x32_bf16 v[76:79], v[160:163], a[152:155], v[76:79]\n"
    "ds_read_b128 a[100:103], v201 offset:41984\n"
    "v_cvt_pk_bf16_f32 v170, v148, v149\n"
    "v_mfma_f32_16x16x32_bf16 v[76:79], v[164:167], a[156:159], v[76:79]\n"
    "v_exp_f32 v150, v150\n"
    "v_mfma_f32_16x16x32_bf16 v[80:83], v[160:163], a[160:163], v[80:83]\n"
    "ds_read_b128 a[104:107], v201 offset:43008\n"
    "v_mfma_f32_16x16x32_bf16 v[80:83], v[164:167], a[164:167], v[80:83]\n"
    "v_exp_f32 v151, v151\n"
    "v_mfma_f32_16x16x32_bf16 v[84:87], v[160:163], a[168:171], v[84:87]\n"
    "ds_read_b128 a[108:111], v201 offset:44032\n"
    "v_cvt_pk_bf16_f32 v171, v150, v151\n"
    "v_mfma_f32_16x16x32_bf16 v[84:87], v[164:167], a[172:175], v[84:87]\n"
    "v_exp_f32 v152, v152\n"
    "v_mfma_f32_16x16x32_bf16 v[88:91], v[160:163], a[176:179], v[88:91]\n"
    "ds_read_b128 a[124:127], v201 offset:48128\n"
    "v_mfma_f32_16x16x32_bf16 v[88:91], v[164:167], a[180:183], v[88:91]\n"
    "v_exp_f32 v153, v153\n"
    "v_mfma_f32_16x16x32_bf16 v[92:95], v[160:163], a[184:187], v[92:95]\n"
    "v_exp_f32 v154, v154\n"
    "v_mfma_f32_16x16x32_bf16 v[92:95], v[164:167], a[188:191], v[92:95]\n"
    "ds_read_b128 a[120:123], v201 offset:47104\n"
    "v_cvt_pk_bf16_f32 v172, v152, v153\n"
    "v_smfmac_f32_16x16x64_bf16 a[204:207], a[192:195], v[160:167], v240\n"
    "s_waitcnt lgkmcnt(4)\n"
    "ds_read_b128 a[116:119], v201 offset:46080\n"
    "v_mfma_f32_16x16x32_bf16 v[128:131], a[64:67], a[0:3], v[176:179]\n"
    "v_exp_f32 v155, v155\n"
    "v_mfma_f32_16x16x32_bf16 v[128:131], a[68:71], a[4:7], v[128:131]\n"
    "v_exp_f32 v156, v156\n"
    "v_mfma_f32_16x16x32_bf16 v[128:131], a[72:75], a[8:11], v[128:131]\n"
    "ds_read_b128 a[112:115], v201 offset:45056\n"
    "v_cvt_pk_bf16_f32 v173, v154, v155\n"
    "v_mfma_f32_16x16x32_bf16 v[128:131], a[76:79], a[12:15], v[128:131]\n"
    "v_exp_f32 v157, v157\n"
    "v_mfma_f32_16x16x32_bf16 v[132:135], a[92:95], a[12:15], v[176:179]\n"
    "v_cvt_pk_bf16_f32 v174, v156, v157\n"
    "v_mfma_f32_16x16x32_bf16 v[132:135], a[88:91], a[8:11], v[132:135]\n"
    "v_exp_f32 v158, v158\n"
    "v_mfma_f32_16x16x32_bf16 v[132:135], a[84:87], a[4:7], v[132:135]\n"
    "v_exp_f32 v159, v159\n"
    "v_mfma_f32_16x16x32_bf16 v[132:135], a[80:83], a[0:3], v[132:135]\n"
    "v_cvt_pk_bf16_f32 v175, v158, v159\n"
    "v_mfma_f32_16x16x32_bf16 v[136:139], a[96:99], a[0:3], v[176:179]\n"
    "v_mfma_f32_16x16x32_bf16 v[136:139], a[100:103], a[4:7], v[136:139]\n"
    "s_waitcnt lgkmcnt(1)\n"
    "v_mfma_f32_16x16x32_bf16 v[136:139], a[104:107], a[8:11], v[136:139]\n"
    "v_exp_f32 v128, v128\n"
    "v_mfma_f32_16x16x32_bf16 v[136:139], a[108:111], a[12:15], v[136:139]\n"
    "v_lshl_add_u32 v204, v201, 3, s6\n"
    "v_min_u32 v204, 11f-12f, v204\n"
    "v_mfma_f32_16x16x32_bf16 v[140:143], a[124:127], a[12:15], v[176:179]\n"
    "v_exp_f32 v129, v129\n"
    "v_mfma_f32_16x16x32_bf16 v[140:143], a[120:123], a[8:11], v[140:143]\n"
    "v_cvt_pk_bf16_f32 v160, v128, v129\n"
    "v_mfma_f32_16x16x32_bf16 v[140:143], a[116:119], a[4:7], v[140:143]\n"
    "s_waitcnt lgkmcnt(0)\n"
    "v_exp_f32 v130, v130\n"
    "v_mfma_f32_16x16x32_bf16 v[140:143], a[112:115], a[0:3], v[140:143]\n"
    "v_exp_f32 v131, v131\n"
    "v_mfma_f32_16x16x32_bf16 v[96:99], v[168:171], a[128:131], v[96:99]\n"
    "v_cvt_pk_bf16_f32 v161, v130, v131\n"
    "v_mfma_f32_16x16x32_bf16 v[96:99], v[172:175], a[132:135], v[96:99]\n"
    "ds_read_b128 a[128:131], v201 offset:49152\n"
    "v_mfma_f32_16x16x32_bf16 v[100:103], v[168:171], a[136:139], v[100:103]\n"
    "v_exp_f32 v132, v132\n"
    "v_mfma_f32_16x16x32_bf16 v[100:103], v[172:175], a[140:143], v[100:103]\n"
    "ds_read_b128 a[132:135], v201 offset:57344\n"
    "v_mfma_f32_16x16x32_bf16 v[104:107], v[168:171], a[144:147], v[104:107]\n"
    "v_exp_f32 v133, v133\n"
    "v_mfma_f32_16x16x32_bf16 v[104:107], v[172:175], a[148:151], v[104:107]\n"
    "ds_read_b128 a[136:139], v201 offset:50176\n"
    "v_cvt_pk_bf16_f32 v162, v132, v133\n"
    "v_mfma_f32_16x16x32_bf16 v[108:111], v[168:171], a[152:155], v[108:111]\n"
    "v_exp_f32 v134, v134\n"
    "v_mfma_f32_16x16x32_bf16 v[108:111], v[172:175], a[156:159], v[108:111]\n"
    "v_exp_f32 v135, v135\n"
    "v_mfma_f32_16x16x32_bf16 v[112:115], v[168:171], a[160:163], v[112:115]\n"
    "ds_read_b128 a[140:143], v201 offset:58368\n"
    "v_cvt_pk_bf16_f32 v163, v134, v135\n"
    "v_mfma_f32_16x16x32_bf16 v[112:115], v[172:175], a[164:167], v[112:115]\n"
    "v_exp_f32 v136, v136\n"
    "v_mfma_f32_16x16x32_bf16 v[116:119], v[168:171], a[168:171], v[116:119]\n"
    "ds_read_b128 a[144:147], v201 offset:51200\n"
    "v_mfma_f32_16x16x32_bf16 v[116:119], v[172:175], a[172:175], v[116:119]\n"
    "v_exp_f32 v137, v137\n"
    "v_mfma_f32_16x16x32_bf16 v[120:123], v[168:171], a[176:179], v[120:123]\n"
    "ds_read_b128 a[148:151], v201 offset:59392\n"
    "v_cvt_pk_bf16_f32 v164, v136, v137\n"
    "v_mfma_f32_16x16x32_bf16 v[120:123], v[172:175], a[180:183], v[120:123]\n"
    "v_exp_f32 v138, v138\n"
    "v_mfma_f32_16x16x32_bf16 v[124:127], v[168:171], a[184:187], v[124:127]\n"
    "ds_read_b128 a[152:155], v201 offset:52224\n"
    "v_mfma_f32_16x16x32_bf16 v[124:127], v[172:175], a[188:191], v[124:127]\n"
    "v_exp_f32 v139, v139\n"
    "v_smfmac_f32_16x16x64_bf16 a[208:211], a[192:195], v[168:175], v240\n"
    "ds_read_b128 a[156:159], v201 offset:60416\n"
    "v_mfma_f32_16x16x32_bf16 v[144:147], a[64:67], a[16:19], v[180:183]\n"
    "v_cvt_pk_bf16_f32 v165, v138, v139\n"
    "v_mfma_f32_16x16x32_bf16 v[144:147], a[68:71], a[20:23], v[144:147]\n"
    "v_exp_f32 v140, v140\n"
    "v_mfma_f32_16x16x32_bf16 v[144:147], a[72:75], a[24:27], v[144:147]\n"
    "ds_read_b128 a[160:163], v201 offset:53248\n"
    "v_mfma_f32_16x16x32_bf16 v[144:147], a[76:79], a[28:31], v[144:147]\n"
    "v_exp_f32 v141, v141\n"
    "v_mfma_f32_16x16x32_bf16 v[148:151], a[92:95], a[28:31], v[180:183]\n"
    "ds_read_b128 a[164:167], v201 offset:61440\n"
    "v_cvt_pk_bf16_f32 v166, v140, v141\n"
    "v_mfma_f32_16x16x32_bf16 v[148:151], a[88:91], a[24:27], v[148:151]\n"
    "v_exp_f32 v142, v142\n"
    "v_mfma_f32_16x16x32_bf16 v[148:151], a[84:87], a[20:23], v[148:151]\n"
    "ds_read_b128 a[168:171], v201 offset:54272\n"
    "v_mfma_f32_16x16x32_bf16 v[148:151], a[80:83], a[16:19], v[148:151]\n"
    "global_load_dwordx2 v[252:253], v245, s[66:67] offset:0 sc1\n"
    "v_mfma_f32_16x16x32_bf16 v[152:155], a[96:99], a[16:19], v[180:183]\n"
    "s_getpc_b64 s[48:49]\n"
    "12:\n"
    "global_load_dword v205, v204, s[48:49]\n"
    "v_mfma_f32_16x16x32_bf16 v[152:155], a[100:103], a[20:23], v[152:155]\n"
    "ds_read_b128 a[172:175], v201 offset:62464\n"
    "v_mfma_f32_16x16x32_bf16 v[152:155], a[104:107], a[24:27], v[152:155]\n"
    "v_exp_f32 v143, v143\n"
    "v_mfma_f32_16x16x32_bf16 v[152:155], a[108:111], a[28:31], v[152:155]\n"
    "ds_read_b128 a[176:179], v201 offset:55296\n"
    "v_cvt_pk_bf16_f32 v167, v142, v143\n"
    "v_mfma_f32_16x16x32_bf16 v[156:159], a[124:127], a[28:31], v[180:183]\n"
    "v_exp_f32 v144, v144\n"
    "v_mfma_f32_16x16x32_bf16 v[156:159], a[120:123], a[24:27], v[156:159]\n"
    "v_exp_f32 v145, v145\n"
    "v_mfma_f32_16x16x32_bf16 v[156:159], a[116:119], a[20:23], v[156:159]\n"
    "ds_read_b128 a[180:183], v201 offset:63488\n"
    "v_cvt_pk_bf16_f32 v168, v144, v145\n"
    "v_mfma_f32_16x16x32_bf16 v[156:159], a[112:115], a[16:19], v[156:159]\n"
    "s_waitcnt lgkmcnt(3)\n"
    "v_exp_f32 v146, v146\n"
    "v_mfma_f32_16x16x32_bf16 v[0:3], v[160:163], a[128:131], v[0:3]\n"
    "ds_read_b128 a[184:187], v201 offset:56320\n"
    "v_mfma_f32_16x16x32_bf16 v[0:3], v[164:167], a[132:135], v[0:3]\n"
    "v_exp_f32 v147, v147\n"
    "v_mfma_f32_16x16x32_bf16 v[4:7], v[160:163], a[136:139], v[4:7]\n"
    "ds_read_b128 a[188:191], v201 offset:64512\n"
    "v_cvt_pk_bf16_f32 v169, v146, v147\n"
    "v_mfma_f32_16x16x32_bf16 v[4:7], v[164:167], a[140:143], v[4:7]\n"
    "v_exp_f32 v148, v148\n"
    "v_mfma_f32_16x16x32_bf16 v[8:11], v[160:163], a[144:147], v[8:11]\n"
    "v_exp_f32 v149, v149\n"
    "v_mfma_f32_16x16x32_bf16 v[8:11], v[164:167], a[148:151], v[8:11]\n"
    "v_cvt_pk_bf16_f32 v170, v148, v149\n"
    "v_mfma_f32_16x16x32_bf16 v[12:15], v[160:163], a[152:155], v[12:15]\n"
    "v_exp_f32 v150, v150\n"
    "v_mfma_f32_16x16x32_bf16 v[12:15], v[164:167], a[156:159], v[12:15]\n"
    "v_exp_f32 v151, v151\n"
    "v_mfma_f32_16x16x32_bf16 v[16:19], v[160:163], a[160:163], v[16:19]\n"
    "v_cvt_pk_bf16_f32 v171, v150, v151\n"
    "v_mfma_f32_16x16x32_bf16 v[16:19], v[164:167], a[164:167], v[16:19]\n"
    "v_exp_f32 v152, v152\n"
    "v_mfma_f32_16x16x32_bf16 v[20:23], v[160:163], a[168:171], v[20:23]\n"
    "s_waitcnt lgkmcnt(1)\n"
    "v_exp_f32 v153, v153\n"
    "v_mfma_f32_16x16x32_bf16 v[20:23], v[164:167], a[172:175], v[20:23]\n"
    "v_cvt_pk_bf16_f32 v172, v152, v153\n"
    "v_mfma_f32_16x16x32_bf16 v[24:27], v[160:163], a[176:179], v[24:27]\n"
    "v_exp_f32 v154, v154\n"
    "v_mfma_f32_16x16x32_bf16 v[24:27], v[164:167], a[180:183], v[24:27]\n"
    "v_exp_f32 v155, v155\n"
    "v_mfma_f32_16x16x32_bf16 v[28:31], v[160:163], a[184:187], v[28:31]\n"
    "s_waitcnt lgkmcnt(0)\n"
    "v_cvt_pk_bf16_f32 v173, v154, v155\n"
    "v_mfma_f32_16x16x32_bf16 v[28:31], v[164:167], a[188:191], v[28:31]\n"
    "v_exp_f32 v156, v156\n"
    "v_smfmac_f32_16x16x64_bf16 a[196:199], a[192:195], v[160:167], v240\n"
    "s_add_u32 m0, s6, 0x8000\n"
    "v_mfma_f32_16x16x32_bf16 v[128:131], a[64:67], a[32:35], v[184:187]\n"
    "v_exp_f32 v157, v157\n"
    "v_mfma_f32_16x16x32_bf16 v[128:131], a[68:71], a[36:39], v[128:131]\n"
    "v_cvt_pk_bf16_f32 v174, v156, v157\n"
    "v_mfma_f32_16x16x32_bf16 v[128:131], a[72:75], a[40:43], v[128:131]\n"
    "s_barrier\n"
    "v_mfma_f32_16x16x32_bf16 v[128:131], a[76:79], a[44:47], v[128:131]\n"
    "global_load_lds_dwordx4 v201, s[40:41] offset:0 sc1\n"
    "v_mfma_f32_16x16x32_bf16 v[132:135], a[92:95], a[44:47], v[184:187]\n"
    "v_exp_f32 v158, v158\n"
    "v_mfma_f32_16x16x32_bf16 v[132:135], a[88:91], a[40:43], v[132:135]\n"
    "v_exp_f32 v159, v159\n"
    "v_mfma_f32_16x16x32_bf16 v[132:135], a[84:87], a[36:39], v[132:135]\n"
    "v_cvt_pk_bf16_f32 v175, v158, v159\n"
    "v_mfma_f32_16x16x32_bf16 v[132:135], a[80:83], a[32:35], v[132:135]\n"
    "v_mfma_f32_16x16x32_bf16 v[136:139], a[96:99], a[32:35], v[184:187]\n"
    "v_mfma_f32_16x16x32_bf16 v[136:139], a[100:103], a[36:39], v[136:139]\n"
    "v_mfma_f32_16x16x32_bf16 v[136:139], a[104:107], a[40:43], v[136:139]\n"
    "global_load_lds_dwordx4 v201, s[40:41] offset:1024 sc1\n"
    "v_mfma_f32_16x16x32_bf16 v[136:139], a[108:111], a[44:47], v[136:139]\n"
    "v_exp_f32 v128, v128\n"
    "v_mfma_f32_16x16x32_bf16 v[140:143], a[124:127], a[44:47], v[184:187]\n"
    "v_exp_f32 v129, v129\n"
    "v_mfma_f32_16x16x32_bf16 v[140:143], a[120:123], a[40:43], v[140:143]\n"
    "v_cvt_pk_bf16_f32 v160, v128, v129\n"
    "v_mfma_f32_16x16x32_bf16 v[140:143], a[116:119], a[36:39], v[140:143]\n"
    "v_exp_f32 v130, v130\n"
    "v_mfma_f32_16x16x32_bf16 v[140:143], a[112:115], a[32:35], v[140:143]\n"
    "v_exp_f32 v131, v131\n"
    "v_mfma_f32_16x16x32_bf16 v[32:35], v[168:171], a[128:131], v[32:35]\n"
    "v_cvt_pk_bf16_f32 v161, v130, v131\n"
    "v_mfma_f32_16x16x32_bf16 v[32:35], v[172:175], a[132:135], v[32:35]\n"
    "global_load_lds_dwordx4 v201, s[40:41] offset:2048 sc1\n"
    "v_mfma_f32_16x16x32_bf16 v[36:39], v[168:171], a[136:139], v[36:39]\n"
    "global_load_lds_dwordx4 v201, s[40:41] offset:3072 sc1\n"
    "v_mfma_f32_16x16x32_bf16 v[36:39], v[172:175], a[140:143], v[36:39]\n"
    "s_add_u32 m0, s6, 0x9000\n"
    "v_exp_f32 v132, v132\n"
    "v_mfma_f32_16x16x32_bf16 v[40:43], v[168:171], a[144:147], v[40:43]\n"
    "v_exp_f32 v133, v133\n"
    "v_mfma_f32_16x16x32_bf16 v[40:43], v[172:175], a[148:151], v[40:43]\n"
    "v_cvt_pk_bf16_f32 v162, v132, v133\n"
    "v_mfma_f32_16x16x32_bf16 v[44:47], v[168:171], a[152:155], v[44:47]\n"
    "v_exp_f32 v134, v134\n"
    "v_mfma_f32_16x16x32_bf16 v[44:47], v[172:175], a[156:159], v[44:47]\n"
    "v_exp_f32 v135, v135\n"
    "v_mfma_f32_16x16x32_bf16 v[48:51], v[168:171], a[160:163], v[48:51]\n"
    "v_cvt_pk_bf16_f32 v163, v134, v135\n"
    "v_mfma_f32_16x16x32_bf16 v[48:51], v[172:175], a[164:167], v[48:51]\n"
    "global_load_lds_dwordx4 v202, s[40:41] offset:0 sc1\n"
    "v_mfma_f32_16x16x32_bf16 v[52:55], v[168:171], a[168:171], v[52:55]\n"
    "v_exp_f32 v136, v136\n"
    "v_mfma_f32_16x16x32_bf16 v[52:55], v[172:175], a[172:175], v[52:55]\n"
    "v_exp_f32 v137, v137\n"
    "v_mfma_f32_16x16x32_bf16 v[56:59], v[168:171], a[176:179], v[56:59]\n"
    "v_cvt_pk_bf16_f32 v164, v136, v137\n"
    "v_mfma_f32_16x16x32_bf16 v[56:59], v[172:175], a[180:183], v[56:59]\n"
    "v_exp_f32 v138, v138\n"
    "v_mfma_f32_16x16x32_bf16 v[60:63], v[168:171], a[184:187], v[60:63]\n"
    "v_exp_f32 v139, v139\n"
    "v_mfma_f32_16x16x32_bf16 v[60:63], v[172:175], a[188:191], v[60:63]\n"
    "v_cvt_pk_bf16_f32 v165, v138, v139\n"
    "v_smfmac_f32_16x16x64_bf16 a[200:203], a[192:195], v[168:175], v240\n"
    "v_mfma_f32_16x16x32_bf16 v[144:147], a[64:67], a[48:51], v[188:191]\n"
    "global_load_lds_dwordx4 v202, s[40:41] offset:1024 sc1\n"
    "v_mfma_f32_16x16x32_bf16 v[144:147], a[68:71], a[52:55], v[144:147]\n"
    "v_exp_f32 v140, v140\n"
    "v_mfma_f32_16x16x32_bf16 v[144:147], a[72:75], a[56:59], v[144:147]\n"
    "ds_read_b128 a[64:67], v203 offset:0\n"
    "v_mfma_f32_16x16x32_bf16 v[144:147], a[76:79], a[60:63], v[144:147]\n"
    "v_exp_f32 v141, v141\n"
    "v_mfma_f32_16x16x32_bf16 v[148:151], a[92:95], a[60:63], v[188:191]\n"
    "ds_read_b128 a[68:71], v203 offset:1024\n"
    "v_cvt_pk_bf16_f32 v166, v140, v141\n"
    "v_mfma_f32_16x16x32_bf16 v[148:151], a[88:91], a[56:59], v[148:151]\n"
    "v_exp_f32 v142, v142\n"
    "v_mfma_f32_16x16x32_bf16 v[148:151], a[84:87], a[52:55], v[148:151]\n"
    "ds_read_b128 a[72:75], v203 offset:2048\n"
    "v_mfma_f32_16x16x32_bf16 v[148:151], a[80:83], a[48:51], v[148:151]\n"
    "global_load_lds_dwordx4 v202, s[40:41] offset:2048 sc1\n"
    "v_mfma_f32_16x16x32_bf16 v[152:155], a[96:99], a[48:51], v[188:191]\n"
    "v_exp_f32 v143, v143\n"
    "v_mfma_f32_16x16x32_bf16 v[152:155], a[100:103], a[52:55], v[152:155]\n"
    "ds_read_b128 a[76:79], v203 offset:3072\n"
    "v_cvt_pk_bf16_f32 v167, v142, v143\n"
    "v_mfma_f32_16x16x32_bf16 v[152:155], a[104:107], a[56:59], v[152:155]\n"
    "v_exp_f32 v144, v144\n"
    "v_mfma_f32_16x16x32_bf16 v[152:155], a[108:111], a[60:63], v[152:155]\n"
    "ds_read_b128 a[92:95], v203 offset:7168\n"
    "v_mfma_f32_16x16x32_bf16 v[156:159], a[124:127], a[60:63], v[188:191]\n"
    "v_exp_f32 v145, v145\n"
    "v_mfma_f32_16x16x32_bf16 v[156:159], a[120:123], a[56:59], v[156:159]\n"
    "ds_read_b128 a[88:91], v203 offset:6144\n"
    "v_cvt_pk_bf16_f32 v168, v144, v145\n"
    "v_mfma_f32_16x16x32_bf16 v[156:159], a[116:119], a[52:55], v[156:159]\n"
    "global_load_lds_dwordx4 v202, s[40:41] offset:3072 sc1\n"
    "v_mfma_f32_16x16x32_bf16 v[156:159], a[112:115], a[48:51], v[156:159]\n"
    "s_add_u32 s40, s40, 0x8000\n"
    "ds_read_b128 a[84:87], v203 offset:5120\n"
    "v_mfma_f32_16x16x32_bf16 v[64:67], v[160:163], a[128:131], v[64:67]\n"
    "s_addc_u32 s41, s41, 0\n"
    "v_exp_f32 v146, v146\n"
    "v_mfma_f32_16x16x32_bf16 v[64:67], v[164:167], a[132:135], v[64:67]\n"
    "s_waitcnt vmcnt(9)\n"
    "ds_read_b128 a[80:83], v203 offset:4096\n"
    "v_mfma_f32_16x16x32_bf16 v[68:71], v[160:163], a[136:139], v[68:71]\n"
    "v_cmp_ne_u64 vcc, v[252:253], v[246:247]\n"
    "s_cbranch_vccz 31f\n"
    "s_mov_b32 s48, 20000\n"
    "30:\n"
    "global_load_dwordx2 v[252:253], v245, s[66:67] offset:0 sc1\n"
    " s_waitcnt vmcnt(0)\n"
    "v_cmp_ne_u64 vcc, v[252:253], v[246:247]\n"
    "s_cbranch_vccz 31f\n"
    "s_sleep 1\n"
    "s_sub_u32 s48, s48, 1\n"
    "s_cbranch_scc0 30b\n"
    "s_mov_b32 s34, 1\n"
    "31:\n"
    "s_lshl_b32 s48, %[w], 2\n"
    "s_add_u32 s48, s48, 0x1b000\n"
    "v_mov_b32 v252, s48\n"
    "v_mov_b32 v253, s34\n"
    "ds_write_b32 v252, v253\n"
    "v_exp_f32 v147, v147\n"
    "v_mfma_f32_16x16x32_bf16 v[68:71], v[164:167], a[140:143], v[68:71]\n"
    "v_exp_f32 v148, v148\n"
    "v_mfma_f32_16x16x32_bf16 v[72:75], v[160:163], a[144:147], v[72:75]\n"
    "ds_read_b128 a[96:99], v203 offset:8192\n"
    "v_cvt_pk_bf16_f32 v169, v146, v147\n"
    "v_mfma_f32_16x16x32_bf16 v[72:75], v[164:167], a[148:151], v[72:75]\n"
    "v_exp_f32 v149, v149\n"
    "v_mfma_f32_16x16x32_bf16 v[76:79], v[160:163], a[152:155], v[76:79]\n"
    "ds_read_b128 a[100:103], v203 offset:9216\n"
    "v_cvt_pk_bf16_f32 v170, v148, v149\n"
    "v_mfma_f32_16x16x32_bf16 v[76:79], v[164:167], a[156:159], v[76:79]\n"
    "v_exp_f32 v150, v150\n"
    "v_mfma_f32_16x16x32_bf16 v[80:83], v[160:163], a[160:163], v[80:83]\n"
    "ds_read_b128 a[104:107], v203 offset:10240\n"
    "v_mfma_f32_16x16x32_bf16 v[80:83], v[164:167], a[164:167], v[80:83]\n"
    "v_exp_f32 v151, v151\n"
    "v_mfma_f32_16x16x32_bf16 v[84:87], v[160:163], a[168:171], v[84:87]\n"
    "ds_read_b128 a[108:111], v203 offset:11264\n"
    "v_cvt_pk_bf16_f32 v171, v150, v151\n"
    "v_mfma_f32_16x16x32_bf16 v[84:87], v[164:167], a[172:175], v[84:87]\n"
    "v_exp_f32 v152, v152\n"
    "v_mfma_f32_16x16x32_bf16 v[88:91], v[160:163], a[176:179], v[88:91]\n"
    "ds_read_b128 a[124:127], v203 offset:15360\n"
    "v_mfma_f32_16x16x32_bf16 v[88:91], v[164:167], a[180:183], v[88:91]\n"
    "v_exp_f32 v153, v153\n"
    "v_mfma_f32_16x16x32_bf16 v[92:95], v[160:163], a[184:187], v[92:95]\n"
    "v_exp_f32 v154, v154\n"
    "v_mfma_f32_16x16x32_bf16 v[92:95], v[164:167], a[188:191], v[92:95]\n"
    "ds_read_b128 a[120:123], v203 offset:14336\n"
    "v_cvt_pk_bf16_f32 v172, v152, v153\n"
    "v_smfmac_f32_16x16x64_bf16 a[204:207], a[192:195], v[160:167], v240\n"
    "s_waitcnt lgkmcnt(10)\n"
    ".p2align 6\n"
    "1:\n"
    "v_mfma_f32_16x16x32_bf16 v[128:131], a[64:67], a[0:3], v[176:179]\n"
    "ds_read_b128 a[116:119], v203 offset:13312\n"
    "v_mfma_f32_16x16x32_bf16 v[128:131], a[68:71], a[4:7], v[128:131]\n"
    "v_exp_f32 v155, v155\n"
    "v_mfma_f32_16x16x32_bf16 v[128:131], a[72:75], a[8:11], v[128:131]\n"
    "ds_read_b128 a[112:115], v203 offset:12288\n"
    "v_cvt_pk_bf16_f32 v173, v154, v155\n"
    "v_mfma_f32_16x16x32_bf16 v[128:131], a[76:79], a[12:15], v[128:131]\n"
    "s_waitcnt lgkmcnt(8)\n"
    "v_exp_f32 v156, v156\n"
    "v_mfma_f32_16x16x32_bf16 v[132:135], a[92:95], a[12:15], v[176:179]\n"
    "204: s_cbranch_execz 205f\n"
    "v_exp_f32 v157, v157\n"
    "v_mfma_f32_16x16x32_bf16 v[132:135], a[88:91], a[8:11], v[132:135]\n"
    "v_exp_f32 v158, v158\n"
    "v_mfma_f32_16x16x32_bf16 v[132:135], a[84:87], a[4:7], v[132:135]\n"
    "v_cvt_pk_bf16_f32 v174, v156, v157\n"
    "v_mfma_f32_16x16x32_bf16 v[132:135], a[80:83], a[0:3], v[132:135]\n"
    "s_waitcnt lgkmcnt(4)\n"
    "v_exp_f32 v159, v159\n"
    "v_mfma_f32_16x16x32_bf16 v[136:139], a[96:99], a[0:3], v[176:179]\n"
    "v_cvt_pk_bf16_f32 v175, v158, v159\n"
    "v_mfma_f32_16x16x32_bf16 v[136:139], a[100:103], a[4:7], v[136:139]\n"
    "v_mfma_f32_16x16x32_bf16 v[136:139], a[104:107], a[8:11], v[136:139]\n"
    "v_exp_f32 v128, v128\n"
    "v_mfma_f32_16x16x32_bf16 v[136:139], a[108:111], a[12:15], v[136:139]\n"
    "s_waitcnt lgkmcnt(0)\n"
    "v_mfma_f32_16x16x32_bf16 v[140:143], a[124:127], a[12:15], v[176:179]\n"
    "v_exp_f32 v129, v129\n"
    "v_mfma_f32_16x16x32_bf16 v[140:143], a[120:123], a[8:11], v[140:143]\n"
    "v_cvt_pk_bf16_f32 v160, v128, v129\n"
    "v_mfma_f32_16x16x32_bf16 v[140:143], a[116:119], a[4:7], v[140:143]\n"
    "v_exp_f32 v130, v130\n"
    "v_mfma_f32_16x16x32_bf16 v[140:143], a[112:115], a[0:3], v[140:143]\n"
    "v_mfma_f32_16x16x32_bf16 v[96:99], v[168:171], a[128:131], v[96:99]\n"
    "v_exp_f32 v131, v131\n"
    "v_mfma_f32_16x16x32_bf16 v[96:99], v[172:175], a[132:135], v[96:99]\n"
    "ds_read_b128 a[128:131], v203 offset:16384\n"
    "v_cvt_pk_bf16_f32 v161, v130, v131\n"
    "v_mfma_f32_16x16x32_bf16 v[100:103], v[168:171], a[136:139], v[100:103]\n"
    "v_exp_f32 v132, v132\n"
    "v_mfma_f32_16x16x32_bf16 v[100:103], v[172:175], a[140:143], v[100:103]\n"
    "ds_read_b128 a[132:135], v203 offset:24576\n"
    "v_mfma_f32_16x16x32_bf16 v[104:107], v[168:171], a[144:147], v[104:107]\n"
    "v_exp_f32 v133, v133\n"
    "v_mfma_f32_16x16x32_bf16 v[104:107], v[172:175], a[148:151], v[104:107]\n"
    "ds_read_b128 a[136:139], v203 offset:17408\n"
    "v_cvt_pk_bf16_f32 v162, v132, v133\n"
    "v_mfma_f32_16x16x32_bf16 v[108:111], v[168:171], a[152:155], v[108:111]\n"
    "v_exp_f32 v134, v134\n"
    "v_mfma_f32_16x16x32_bf16 v[108:111], v[172:175], a[156:159], v[108:111]\n"
    "v_mfma_f32_16x16x32_bf16 v[112:115], v[168:171], a[160:163], v[112:115]\n"
    "v_exp_f32 v135, v135\n"
    "v_mfma_f32_16x16x32_bf16 v[112:115], v[172:175], a[164:167], v[112:115]\n"
    "ds_read_b128 a[140:143], v203 offset:25600\n"
    "v_cvt_pk_bf16_f32 v163, v134, v135\n"
    "v_mfma_f32_16x16x32_bf16 v[116:119], v[168:171], a[168:171], v[116:119]\n"
    "v_exp_f32 v136, v136\n"
    "v_mfma_f32_16x16x32_bf16 v[116:119], v[172:175], a[172:175], v[116:119]\n"
    "ds_read_b128 a[144:147], v203 offset:18432\n"
    "v_mfma_f32_16x16x32_bf16 v[120:123], v[168:171], a[176:179], v[120:123]\n"
    "v_exp_f32 v137, v137\n"
    "v_mfma_f32_16x16x32_bf16 v[120:123], v[172:175], a[180:183], v[120:123]\n"
    "ds_read_b128 a[148:151], v203 offset:26624\n"
    "v_cvt_pk_bf16_f32 v164, v136, v137\n"
    "v_mfma_f32_16x16x32_bf16 v[124:127], v[168:171], a[184:187], v[124:127]\n"
    "v_exp_f32 v138, v138\n"
    "v_mfma_f32_16x16x32_bf16 v[124:127], v[172:175], a[188:191], v[124:127]\n"
    "ds_read_b128 a[152:155], v203 offset:19456\n"
    "v_smfmac_f32_16x16x64_bf16 a[208:211], a[192:195], v[168:175], v240\n"
    "ds_read_b128 a[156:159], v203 offset:27648\n"
    "v_mfma_f32_16x16x32_bf16 v[144:147], a[64:67], a[16:19], v[180:183]\n"
    "v_exp_f32 v139, v139\n"
    "v_mfma_f32_16x16x32_bf16 v[144:147], a[68:71], a[20:23], v[144:147]\n"
    "v_exp_f32 v140, v140\n"
    "v_mfma_f32_16x16x32_bf16 v[144:147], a[72:75], a[24:27], v[144:147]\n"
    "ds_read_b128 a[160:163], v203 offset:20480\n"
    "v_cvt_pk_bf16_f32 v165, v138, v139\n"
    "v_mfma_f32_16x16x32_bf16 v[144:147], a[76:79], a[28:31], v[144:147]\n"
    "v_exp_f32 v141, v141\n"
    "v_mfma_f32_16x16x32_bf16 v[148:151], a[92:95], a[28:31], v[180:183]\n"
    "ds_read_b128 a[164:167], v203 offset:28672\n"
    "v_cvt_pk_bf16_f32 v166, v140, v141\n"
    "v_mfma_f32_16x16x32_bf16 v[148:151], a[88:91], a[24:27], v[148:151]\n"
    "v_exp_f32 v142, v142\n"
    "v_mfma_f32_16x16x32_bf16 v[148:151], a[84:87], a[20:23], v[148:151]\n"
    "ds_read_b128 a[168:171], v203 offset:21504\n"
    "v_mfma_f32_16x16x32_bf16 v[148:151], a[80:83], a[16:19], v[148:151]\n"
    "v_exp_f32 v143, v143\n"
    "v_mfma_f32_16x16x32_bf16 v[152:155], a[96:99], a[16:19], v[180:183]\n"
    "ds_read_b128 a[172:175], v203 offset:29696\n"
    "v_cvt_pk_bf16_f32 v167, v142, v143\n"
    "v_mfma_f32_16x16x32_bf16 v[152:155], a[100:103], a[20:23], v[152:155]\n"
    "v_mfma_f32_16x16x32_bf16 v[152:155], a[104:107], a[24:27], v[152:155]\n"
    "v_exp_f32 v144, v144\n"
    "v_mfma_f32_16x16x32_bf16 v[152:155], a[108:111], a[28:31], v[152:155]\n"
    "ds_read_b128 a[176:179], v203 offset:22528\n"
    "v_mfma_f32_16x16x32_bf16 v[156:159], a[124:127], a[28:31], v[180:183]\n"
    "v_exp_f32 v145, v145\n"
    "v_mfma_f32_16x16x32_bf16 v[156:159], a[120:123], a[24:27], v[156:159]\n"
    "ds_read_b128 a[180:183], v203 offset:30720\n"
    "v_cvt_pk_bf16_f32 v168, v144, v145\n"
    "v_mfma_f32_16x16x32_bf16 v[156:159], a[116:119], a[20:23], v[156:159]\n"
    "v_exp_f32 v146, v146\n"
    "v_mfma_f32_16x16x32_bf16 v[156:159], a[112:115], a[16:19], v[156:159]\n"
    "s_waitcnt lgkmcnt(10)\n"
    "ds_read_b128 a[184:187], v203 offset:23552\n"
    "v_mfma_f32_16x16x32_bf16 v[0:3], v[160:163], a[128:131], v[0:3]\n"
    "v_exp_f32 v147, v147\n"
    "v_mfma_f32_16x16x32_bf16 v[0:3], v[164:167], a[132:135], v[0:3]\n"
    "ds_read_b128 a[188:191], v203 offset:31744\n"
    "v_cvt_pk_bf16_f32 v169, v146, v147\n"
    "v_mfma_f32_16x16x32_bf16 v[4:7], v[160:163], a[136:139], v[4:7]\n"
    "v_exp_f32 v148, v148\n"
    "v_mfma_f32_16x16x32_bf16 v[4:7], v[164:167], a[140:143], v[4:7]\n"
    "s_waitcnt lgkmcnt(8)\n"
    "v_mfma_f32_16x16x32_bf16 v[8:11], v[160:163], a[144:147], v[8:11]\n"
    "v_exp_f32 v149, v149\n"
    "v_mfma_f32_16x16x32_bf16 v[8:11], v[164:167], a[148:151], v[8:11]\n"
    "v_cvt_pk_bf16_f32 v170, v148, v149\n"
    "v_mfma_f32_16x16x32_bf16 v[12:15], v[160:163], a[152:155], v[12:15]\n"
    "v_exp_f32 v150, v150\n"
    "v_mfma_f32_16x16x32_bf16 v[12:15], v[164:167], a[156:159], v[12:15]\n"
    "s_waitcnt lgkmcnt(4)\n"
    "v_mfma_f32_16x16x32_bf16 v[16:19], v[160:163], a[160:163], v[16:19]\n"
    "v_exp_f32 v151, v151\n"
    "v_mfma_f32_16x16x32_bf16 v[16:19], v[164:167], a[164:167], v[16:19]\n"
    "v_cvt_pk_bf16_f32 v171, v150, v151\n"
    "v_mfma_f32_16x16x32_bf16 v[20:23], v[160:163], a[168:171], v[20:23]\n"
    "v_exp_f32 v152, v152\n"
    "v_mfma_f32_16x16x32_bf16 v[20:23], v[164:167], a[172:175], v[20:23]\n"
    "s_waitcnt lgkmcnt(0)\n"
    "v_mfma_f32_16x16x32_bf16 v[24:27], v[160:163], a[176:179], v[24:27]\n"
    "s_add_u32 m0, s6, 0x10000\n"
    "v_exp_f32 v153, v153\n"
    "v_mfma_f32_16x16x32_bf16 v[24:27], v[164:167], a[180:183], v[24:27]\n"
    "v_cvt_pk_bf16_f32 v172, v152, v153\n"
    "v_mfma_f32_16x16x32_bf16 v[28:31], v[160:163], a[184:187], v[28:31]\n"
    "v_exp_f32 v154, v154\n"
    "v_mfma_f32_16x16x32_bf16 v[28:31], v[164:167], a[188:191], v[28:31]\n"
    "v_smfmac_f32_16x16x64_bf16 a[196:199], a[192:195], v[160:167], v240\n"
    "v_mfma_f32_16x16x32_bf16 v[128:131], a[64:67], a[32:35], v[184:187]\n"
    "v_exp_f32 v155, v155\n"
    "v_mfma_f32_16x16x32_bf16 v[128:131], a[68:71], a[36:39], v[128:131]\n"
    "s_waitcnt vmcnt(8)\n"
    "v_cvt_pk_bf16_f32 v173, v154, v155\n"
    "v_mfma_f32_16x16x32_bf16 v[128:131], a[72:75], a[40:43], v[128:131]\n"
    "s_barrier\n"
    "v_mfma_f32_16x16x32_bf16 v[128:131], a[76:79], a[44:47], v[128:131]\n"
    "global_load_lds_dwordx4 v201, s[40:41] offset:0 sc1\n"
    "v_mfma_f32_16x16x32_bf16 v[132:135], a[92:95], a[44:47], v[184:187]\n"
    "v_exp_f32 v156, v156\n"
    "v_mfma_f32_16x16x32_bf16 v[132:135], a[88:91], a[40:43], v[132:135]\n"
    "v_exp_f32 v157, v157\n"
    "v_mfma_f32_16x16x32_bf16 v[132:135], a[84:87], a[36:39], v[132:135]\n"
    "v_exp_f32 v158, v158\n"
    "v_mfma_f32_16x16x32_bf16 v[132:135], a[80:83], a[32:35], v[132:135]\n"
    "v_cvt_pk_bf16_f32 v174, v156, v157\n"
    "v_mfma_f32_16x16x32_bf16 v[136:139], a[96:99], a[32:35], v[184:187]\n"
    "global_load_lds_dwordx4 v201, s[40:41] offset:1024 sc1\n"
    "v_mfma_f32_16x16x32_bf16 v[136:139], a[100:103], a[36:39], v[136:139]\n"
    "v_exp_f32 v159, v159\n"
    "v_mfma_f32_16x16x32_bf16 v[136:139], a[104:107], a[40:43], v[136:139]\n"
    "v_exp_f32 v128, v128\n"
    "v_mfma_f32_16x16x32_bf16 v[136:139], a[108:111], a[44:47], v[136:139]\n"
    "v_cvt_pk_bf16_f32 v175, v158, v159\n"
    "v_mfma_f32_16x16x32_bf16 v[140:143], a[124:127], a[44:47], v[184:187]\n"
    "v_mfma_f32_16x16x32_bf16 v[140:143], a[120:123], a[40:43], v[140:143]\n"
    "global_load_lds_dwordx4 v201, s[40:41] offset:2048 sc1\n"
    "v_mfma_f32_16x16x32_bf16 v[140:143], a[116:119], a[36:39], v[140:143]\n"
    "v_exp_f32 v129, v129\n"
    "v_mfma_f32_16x16x32_bf16 v[140:143], a[112:115], a[32:35], v[140:143]\n"
    "v_exp_f32 v130, v130\n"
    "v_mfma_f32_16x16x32_bf16 v[32:35], v[168:171], a[128:131], v[32:35]\n"
    "v_exp_f32 v131, v131\n"
    "v_mfma_f32_16x16x32_bf16 v[32:35], v[172:175], a[132:135], v[32:35]\n"
    "v_cvt_pk_bf16_f32 v160, v128, v129\n"
    "v_mfma_f32_16x16x32_bf16 v[36:39], v[168:171], a[136:139], v[36:39]\n"
    "global_load_lds_dwordx4 v201, s[40:41] offset:3072 sc1\n"
    "v_mfma_f32_16x16x32_bf16 v[36:39], v[172:175], a[140:143], v[36:39]\n"
    "s_add_u32 m0, s6, 0x11000\n"
    "v_exp_f32 v132, v132\n"
    "v_mfma_f32_16x16x32_bf16 v[40:43], v[168:171], a[144:147], v[40:43]\n"
    "v_exp_f32 v133, v133\n"
    "v_mfma_f32_16x16x32_bf16 v[40:43], v[172:175], a[148:151], v[40:43]\n"
    "v_cvt_pk_bf16_f32 v161, v130, v131\n"
    "v_cvt_pk_bf16_f32 v162, v132, v133\n"
    "v_mfma_f32_16x16x32_bf16 v[44:47], v[168:171], a[152:155], v[44:47]\n"
    "v_mfma_f32_16x16x32_bf16 v[44:47], v[172:175], a[156:159], v[44:47]\n"
    "global_load_lds_dwordx4 v202, s[40:41] offset:0 sc1\n"
    "v_mfma_f32_16x16x32_bf16 v[48:51], v[168:171], a[160:163], v[48:51]\n"
    "v_exp_f32 v134, v134\n"
    "v_mfma_f32_16x16x32_bf16 v[48:51], v[172:175], a[164:167], v[48:51]\n"
    "v_exp_f32 v135, v135\n"
    "v_mfma_f32_16x16x32_bf16 v[52:55], v[168:171], a[168:171], v[52:55]\n"
    "v_exp_f32 v136, v136\n"
    "v_mfma_f32_16x16x32_bf16 v[52:55], v[172:175], a[172:175], v[52:55]\n"
    "v_cvt_pk_bf16_f32 v163, v134, v135\n"
    "v_mfma_f32_16x16x32_bf16 v[56:59], v[168:171], a[176:179], v[56:59]\n"
    "global_load_lds_dwordx4 v202, s[40:41] offset:1024 sc1\n"
    "v_mfma_f32_16x16x32_bf16 v[56:59], v[172:175], a[180:183], v[56:59]\n"
    "v_exp_f32 v137, v137\n"
    "v_mfma_f32_16x16x32_bf16 v[60:63], v[168:171], a[184:187], v[60:63]\n"
    "v_exp_f32 v138, v138\n"
    "v_mfma_f32_16x16x32_bf16 v[60:63], v[172:175], a[188:191], v[60:63]\n"
    "v_cvt_pk_bf16_f32 v164, v136, v137\n"
    "v_smfmac_f32_16x16x64_bf16 a[200:203], a[192:195], v[168:175], v240\n"
    "v_mfma_f32_16x16x32_bf16 v[144:147], a[64:67], a[48:51], v[188:191]\n"
    "v_exp_f32 v139, v139\n"
    "v_mfma_f32_16x16x32_bf16 v[144:147], a[68:71], a[52:55], v[144:147]\n"
    "v_exp_f32 v140, v140\n"
    "v_mfma_f32_16x16x32_bf16 v[144:147], a[72:75], a[56:59], v[144:147]\n"
    "ds_read_b128 a[64:67], v201 offset:0\n"
    "v_mfma_f32_16x16x32_bf16 v[144:147], a[76:79], a[60:63], v[144:147]\n"
    "global_load_lds_dwordx4 v202, s[40:41] offset:2048 sc1\n"
    "v_mfma_f32_16x16x32_bf16 v[148:151], a[92:95], a[60:63], v[188:191]\n"
    "ds_read_b128 a[68:71], v201 offset:1024\n"
    "v_cvt_pk_bf16_f32 v165, v138, v139\n"
    "v_mfma_f32_16x16x32_bf16 v[148:151], a[88:91], a[56:59], v[148:151]\n"
    "v_exp_f32 v141, v141\n"
    "v_mfma_f32_16x16x32_bf16 v[148:151], a[84:87], a[52:55], v[148:151]\n"
    "ds_read_b128 a[72:75], v201 offset:2048\n"
    "v_cvt_pk_bf16_f32 v166, v140, v141\n"
    "v_mfma_f32_16x16x32_bf16 v[148:151], a[80:83], a[48:51], v[148:151]\n"
    "v_exp_f32 v142, v142\n"
    "v_mfma_f32_16x16x32_bf16 v[152:155], a[96:99], a[48:51], v[188:191]\n"
    "v_exp_f32 v143, v143\n"
    "v_mfma_f32_16x16x32_bf16 v[152:155], a[100:103], a[52:55], v[152:155]\n"
    "ds_read_b128 a[76:79], v201 offset:3072\n"
    "v_cvt_pk_bf16_f32 v167, v142, v143\n"
    "v_mfma_f32_16x16x32_bf16 v[152:155], a[104:107], a[56:59], v[152:155]\n"
    "ds_read_b128 a[92:95], v201 offset:7168\n"
    "v_mfma_f32_16x16x32_bf16 v[152:155], a[108:111], a[60:63], v[152:155]\n"
    "global_load_lds_dwordx4 v202, s[40:41] offset:3072 sc1\n"
    "v_mfma_f32_16x16x32_bf16 v[156:159], a[124:127], a[60:63], v[188:191]\n"
    "s_add_u32 s40, s40, 0x8000\n"
    "v_exp_f32 v144, v144\n"
    "v_mfma_f32_16x16x32_bf16 v[156:159], a[120:123], a[56:59], v[156:159]\n"
    "s_addc_u32 s41, s41, 0\n"
    "ds_read_b128 a[88:91], v201 offset:6144\n"
    "v_mfma_f32_16x16x32_bf16 v[156:159], a[116:119], a[52:55], v[156:159]\n"
    "v_exp_f32 v145, v145\n"
    "v_mfma_f32_16x16x32_bf16 v[156:159], a[112:115], a[48:51], v[156:159]\n"
    "ds_read_b128 a[84:87], v201 offset:5120\n"
    "v_cvt_pk_bf16_f32 v168, v144, v145\n"
    "v_mfma_f32_16x16x32_bf16 v[64:67], v[160:163], a[128:131], v[64:67]\n"
    "v_exp_f32 v146, v146\n"
    "v_mfma_f32_16x16x32_bf16 v[64:67], v[164:167], a[132:135], v[64:67]\n"
    "ds_read_b128 a[80:83], v201 offset:4096\n"
    "v_mfma_f32_16x16x32_bf16 v[68:71], v[160:163], a[136:139], v[68:71]\n"
    "v_exp_f32 v147, v147\n"
    "v_mfma_f32_16x16x32_bf16 v[68:71], v[164:167], a[140:143], v[68:71]\n"
    "v_exp_f32 v148, v148\n"
    "v_mfma_f32_16x16x32_bf16 v[72:75], v[160:163], a[144:147], v[72:75]\n"
    "ds_read_b128 a[96:99], v201 offset:8192\n"
    "v_cvt_pk_bf16_f32 v169, v146, v147\n"
    "v_mfma_f32_16x16x32_bf16 v[72:75], v[164:167], a[148:151], v[72:75]\n"
    "v_exp_f32 v149, v149\n"
    "v_mfma_f32_16x16x32_bf16 v[76:79], v[160:163], a[152:155], v[76:79]\n"
    "ds_read_b128 a[100:103], v201 offset:9216\n"
    "v_cvt_pk_bf16_f32 v170, v148, v149\n"
    "v_mfma_f32_16x16x32_bf16 v[76:79], v[164:167], a[156:159], v[76:79]\n"
    "v_exp_f32 v150, v150\n"
    "v_mfma_f32_16x16x32_bf16 v[80:83], v[160:163], a[160:163], v[80:83]\n"
    "ds_read_b128 a[104:107], v201 offset:10240\n"
    "v_mfma_f32_16x16x32_bf16 v[80:83], v[164:167], a[164:167], v[80:83]\n"
    "v_exp_f32 v151, v151\n"
    "v_mfma_f32_16x16x32_bf16 v[84:87], v[160:163], a[168:171], v[84:87]\n"
    "ds_read_b128 a[108:111], v201 offset:11264\n"
    "v_cvt_pk_bf16_f32 v171, v150, v151\n"
    "v_mfma_f32_16x16x32_bf16 v[84:87], v[164:167], a[172:175], v[84:87]\n"
    "v_exp_f32 v152, v152\n"
    "v_mfma_f32_16x16x32_bf16 v[88:91], v[160:163], a[176:179], v[88:91]\n"
    "ds_read_b128 a[124:127], v201 offset:15360\n"
    "v_mfma_f32_16x16x32_bf16 v[88:91], v[164:167], a[180:183], v[88:91]\n"
    "v_exp_f32 v153, v153\n"
    "v_mfma_f32_16x16x32_bf16 v[92:95], v[160:163], a[184:187], v[92:95]\n"
    "v_exp_f32 v154, v154\n"
    "v_mfma_f32_16x16x32_bf16 v[92:95], v[164:167], a[188:191], v[92:95]\n"
    "ds_read_b128 a[120:123], v201 offset:14336\n"
    "v_cvt_pk_bf16_f32 v172, v152, v153\n"
    "v_smfmac_f32_16x16x64_bf16 a[204:207], a[192:195], v[160:167], v240\n"
    "s_waitcnt lgkmcnt(10)\n"
    "v_mfma_f32_16x16x32_bf16 v[128:131], a[64:67], a[0:3], v[176:179]\n"
    "ds_read_b128 a[116:119], v201 offset:13312\n"
    "v_mfma_f32_16x16x32_bf16 v[128:131], a[68:71], a[4:7], v[128:131]\n"
    "v_exp_f32 v155, v155\n"
    "v_mfma_f32_16x16x32_bf16 v[128:131], a[72:75], a[8:11], v[128:131]\n"
    "ds_read_b128 a[112:115], v201 offset:12288\n"
    "v_cvt_pk_bf16_f32 v173, v154, v155\n"
    "v_mfma_f32_16x16x32_bf16 v[128:131], a[76:79], a[12:15], v[128:131]\n"
    "s_waitcnt lgkmcnt(8)\n"
    "v_exp_f32 v156, v156\n"
    "v_mfma_f32_16x16x32_bf16 v[132:135], a[92:95], a[12:15], v[176:179]\n"
    "v_exp_f32 v157, v157\n"
    "v_mfma_f32_16x16x32_bf16 v[132:135], a[88:91], a[8:11], v[132:135]\n"
    "v_exp_f32 v158, v158\n"
    "v_mfma_f32_16x16x32_bf16 v[132:135], a[84:87], a[4:7], v[132:135]\n"
    "v_cvt_pk_bf16_f32 v174, v156, v157\n"
    "v_mfma_f32_16x16x32_bf16 v[132:135], a[80:83], a[0:3], v[132:135]\n"
    "s_waitcnt lgkmcnt(4)\n"
    "v_exp_f32 v159, v159\n"
    "v_mfma_f32_16x16x32_bf16 v[136:139], a[96:99], a[0:3], v[176:179]\n"
    "v_cvt_pk_bf16_f32 v175, v158, v159\n"
    "v_mfma_f32_16x16x32_bf16 v[136:139], a[100:103], a[4:7], v[136:139]\n"
    "v_mfma_f32_16x16x32_bf16 v[136:139], a[104:107], a[8:11], v[136:139]\n"
    "v_exp_f32 v128, v128\n"
    "v_mfma_f32_16x16x32_bf16 v[136:139], a[108:111], a[12:15], v[136:139]\n"
    "s_waitcnt lgkmcnt(0)\n"
    "v_mfma_f32_16x16x32_bf16 v[140:143], a[124:127], a[12:15], v[176:179]\n"
    "v_exp_f32 v129, v129\n"
    "v_mfma_f32_16x16x32_bf16 v[140:143], a[120:123], a[8:11], v[140:143]\n"
    "v_cvt_pk_bf16_f32 v160, v128, v129\n"
    "v_mfma_f32_16x16x32_bf16 v[140:143], a[116:119], a[4:7], v[140:143]\n"
    "v_exp_f32 v130, v130\n"
    "v_mfma_f32_16x16x32_bf16 v[140:143], a[112:115], a[0:3], v[140:143]\n"
    "v_mfma_f32_16x16x32_bf16 v[96:99], v[168:171], a[128:131], v[96:99]\n"
    "v_exp_f32 v131, v131\n"
    "v_mfma_f32_16x16x32_bf16 v[96:99], v[172:175], a[132:135], v[96:99]\n"
    "ds_read_b128 a[128:131], v201 offset:16384\n"
    "v_cvt_pk_bf16_f32 v161, v130, v131\n"
    "v_mfma_f32_16x16x32_bf16 v[100:103], v[168:171], a[136:139], v[100:103]\n"
    "v_exp_f32 v132, v132\n"
    "v_mfma_f32_16x16x32_bf16 v[100:103], v[172:175], a[140:143], v[100:103]\n"
    "ds_read_b128 a[132:135], v201 offset:24576\n"
    "v_mfma_f32_16x16x32_bf16 v[104:107], v[168:171], a[144:147], v[104:107]\n"
    "v_exp_f32 v133, v133\n"
    "v_mfma_f32_16x16x32_bf16 v[104:107], v[172:175], a[148:151], v[104:107]\n"
    "ds_read_b128 a[136:139], v201 offset:17408\n"
    "v_cvt_pk_bf16_f32 v162, v132, v133\n"
    "v_mfma_f32_16x16x32_bf16 v[108:111], v[168:171], a[152:155], v[108:111]\n"
    "v_exp_f32 v134, v134\n"
    "v_mfma_f32_16x16x32_bf16 v[108:111], v[172:175], a[156:159], v[108:111]\n"
    "v_mfma_f32_16x16x32_bf16 v[112:115], v[168:171], a[160:163], v[112:115]\n"
    "v_exp_f32 v135, v135\n"
    "v_mfma_f32_16x16x32_bf16 v[112:115], v[172:175], a[164:167], v[112:115]\n"
    "ds_read_b128 a[140:143], v201 offset:25600\n"
    "v_cvt_pk_bf16_f32 v163, v134, v135\n"
    "v_mfma_f32_16x16x32_bf16 v[116:119], v[168:171], a[168:171], v[116:119]\n"
    "v_exp_f32 v136, v136\n"
    "v_mfma_f32_16x16x32_bf16 v[116:119], v[172:175], a[172:175], v[116:119]\n"
    "ds_read_b128 a[144:147], v201 offset:18432\n"
    "v_mfma_f32_16x16x32_bf16 v[120:123], v[168:171], a[176:179], v[120:123]\n"
    "v_exp_f32 v137, v137\n"
    "v_mfma_f32_16x16x32_bf16 v[120:123], v[172:175], a[180:183], v[120:123]\n"
    "ds_read_b128 a[148:151], v201 offset:26624\n"
    "v_cvt_pk_bf16_f32 v164, v136, v137\n"
    "v_mfma_f32_16x16x32_bf16 v[124:127], v[168:171], a[184:187], v[124:127]\n"
    "v_exp_f32 v138, v138\n"
    "v_mfma_f32_16x16x32_bf16 v[124:127], v[172:175], a[188:191], v[124:127]\n"
    "ds_read_b128 a[152:155], v201 offset:19456\n"
    "v_smfmac_f32_16x16x64_bf16 a[208:211], a[192:195], v[168:175], v240\n"
    "ds_read_b128 a[156:159], v201 offset:27648\n"
    "v_mfma_f32_16x16x32_bf16 v[144:147], a[64:67], a[16:19], v[180:183]\n"
    "v_exp_f32 v139, v139\n"
    "v_mfma_f32_16x16x32_bf16 v[144:147], a[68:71], a[20:23], v[144:147]\n"
    "v_exp_f32 v140, v140\n"
    "v_mfma_f32_16x16x32_bf16 v[144:147], a[72:75], a[24:27], v[144:147]\n"
    "ds_read_b128 a[160:163], v201 offset:20480\n"
    "v_cvt_pk_bf16_f32 v165, v138, v139\n"
    "v_mfma_f32_16x16x32_bf16 v[144:147], a[76:79], a[28:31], v[144:147]\n"
    "v_exp_f32 v141, v141\n"
    "v_mfma_f32_16x16x32_bf16 v[148:151], a[92:95], a[28:31], v[180:183]\n"
    "ds_read_b128 a[164:167], v201 offset:28672\n"
    "v_cvt_pk_bf16_f32 v166, v140, v141\n"
    "v_mfma_f32_16x16x32_bf16 v[148:151], a[88:91], a[24:27], v[148:151]\n"
    "v_exp_f32 v142, v142\n"
    "v_mfma_f32_16x16x32_bf16 v[148:151], a[84:87], a[20:23], v[148:151]\n"
    "ds_read_b128 a[168:171], v201 offset:21504\n"
    "v_mfma_f32_16x16x32_bf16 v[148:151], a[80:83], a[16:19], v[148:151]\n"
    "v_exp_f32 v143, v143\n"
    "v_mfma_f32_16x16x32_bf16 v[152:155], a[96:99], a[16:19], v[180:183]\n"
    "ds_read_b128 a[172:175], v201 offset:29696\n"
    "v_cvt_pk_bf16_f32 v167, v142, v143\n"
    "v_mfma_f32_16x16x32_bf16 v[152:155], a[100:103], a[20:23], v[152:155]\n"
    "v_mfma_f32_16x16x32_bf16 v[152:155], a[104:107], a[24:27], v[152:155]\n"
    "v_exp_f32 v144, v144\n"
    "v_mfma_f32_16x16x32_bf16 v[152:155], a[108:111], a[28:31], v[152:155]\n"
    "ds_read_b128 a[176:179], v201 offset:22528\n"
    "v_mfma_f32_16x16x32_bf16 v[156:159], a[124:127], a[28:31], v[180:183]\n"
    "v_exp_f32 v145, v145\n"
    "v_mfma_f32_16x16x32_bf16 v[156:159], a[120:123], a[24:27], v[156:159]\n"
    "ds_read_b128 a[180:183], v201 offset:30720\n"
    "v_cvt_pk_bf16_f32 v168, v144, v145\n"
    "v_mfma_f32_16x16x32_bf16 v[156:159], a[116:119], a[20:23], v[156:159]\n"
    "v_exp_f32 v146, v146\n"
    "v_mfma_f32_16x16x32_bf16 v[156:159], a[112:115], a[16:19], v[156:159]\n"
    "s_waitcnt lgkmcnt(10)\n"
    "ds_read_b128 a[184:187], v201 offset:23552\n"
    "v_mfma_f32_16x16x32_bf16 v[0:3], v[160:163], a[128:131], v[0:3]\n"
    "v_exp_f32 v147, v147\n"
    "v_mfma_f32_16x16x32_bf16 v[0:3], v[164:167], a[132:135], v[0:3]\n"
    "ds_read_b128 a[188:191], v201 offset:31744\n"
    "v_cvt_pk_bf16_f32 v169, v146, v147\n"
    "v_mfma_f32_16x16x32_bf16 v[4:7], v[160:163], a[136:139], v[4:7]\n"
    "v_exp_f32 v148, v148\n"
    "v_mfma_f32_16x16x32_bf16 v[4:7], v[164:167], a[140:143], v[4:7]\n"
    "s_waitcnt lgkmcnt(8)\n"
    "v_mfma_f32_16x16x32_bf16 v[8:11], v[160:163], a[144:147], v[8:11]\n"
    "v_exp_f32 v149, v149\n"
    "v_mfma_f32_16x16x32_bf16 v[8:11], v[164:167], a[148:151], v[8:11]\n"
    "v_cvt_pk_bf16_f32 v170, v148, v149\n"
    "v_mfma_f32_16x16x32_bf16 v[12:15], v[160:163], a[152:155], v[12:15]\n"
    "v_exp_f32 v150, v150\n"
    "v_mfma_f32_16x16x32_bf16 v[12:15], v[164:167], a[156:159], v[12:15]\n"
    "s_waitcnt lgkmcnt(4)\n"
    "v_mfma_f32_16x16x32_bf16 v[16:19], v[160:163], a[160:163], v[16:19]\n"
    "v_exp_f32 v151, v151\n"
    "v_mfma_f32_16x16x32_bf16 v[16:19], v[164:167], a[164:167], v[16:19]\n"
    "v_cvt_pk_bf16_f32 v171, v150, v151\n"
    "v_mfma_f32_16x16x32_bf16 v[20:23], v[160:163], a[168:171], v[20:23]\n"
    "v_exp_f32 v152, v152\n"
    "v_mfma_f32_16x16x32_bf16 v[20:23], v[164:167], a[172:175], v[20:23]\n"
    "s_waitcnt lgkmcnt(0)\n"
    "v_mfma_f32_16x16x32_bf16 v[24:27], v[160:163], a[176:179], v[24:27]\n"
    "s_add_u32 m0, s6, 0x0\n"
    "v_exp_f32 v153, v153\n"
    "v_mfma_f32_16x16x32_bf16 v[24:27], v[164:167], a[180:183], v[24:27]\n"
    "v_cvt_pk_bf16_f32 v172, v152, v153\n"
    "v_mfma_f32_16x16x32_bf16 v[28:31], v[160:163], a[184:187], v[28:31]\n"
    "v_exp_f32 v154, v154\n"
    "v_mfma_f32_16x16x32_bf16 v[28:31], v[164:167], a[188:191], v[28:31]\n"
    "v_smfmac_f32_16x16x64_bf16 a[196:199], a[192:195], v[160:167], v240\n"
    "v_mfma_f32_16x16x32_bf16 v[128:131], a[64:67], a[32:35], v[184:187]\n"
    "v_exp_f32 v155, v155\n"
    "v_mfma_f32_16x16x32_bf16 v[128:131], a[68:71], a[36:39], v[128:131]\n"
    "s_waitcnt vmcnt(8)\n"
    "v_cvt_pk_bf16_f32 v173, v154, v155\n"
    "v_mfma_f32_16x16x32_bf16 v[128:131], a[72:75], a[40:43], v[128:131]\n"
    "s_barrier\n"
    "v_mfma_f32_16x16x32_bf16 v[128:131], a[76:79], a[44:47], v[128:131]\n"
    "global_load_lds_dwordx4 v201, s[40:41] offset:0 sc1\n"
    "v_mfma_f32_16x16x32_bf16 v[132:135], a[92:95], a[44:47], v[184:187]\n"
    "v_exp_f32 v156, v156\n"
    "v_mfma_f32_16x16x32_bf16 v[132:135], a[88:91], a[40:43], v[132:135]\n"
    "v_exp_f32 v157, v157\n"
    "v_mfma_f32_16x16x32_bf16 v[132:135], a[84:87], a[36:39], v[132:135]\n"
    "v_exp_f32 v158, v158\n"
    "v_mfma_f32_16x16x32_bf16 v[132:135], a[80:83], a[32:35], v[132:135]\n"
    "v_cvt_pk_bf16_f32 v174, v156, v157\n"
    "v_mfma_f32_16x16x32_bf16 v[136:139], a[96:99], a[32:35], v[184:187]\n"
    "global_load_lds_dwordx4 v201, s[40:41] offset:1024 sc1\n"
    "v_mfma_f32_16x16x32_bf16 v[136:139], a[100:103], a[36:39], v[136:139]\n"
    "v_exp_f32 v159, v159\n"
    "v_mfma_f32_16x16x32_bf16 v[136:139], a[104:107], a[40:43], v[136:139]\n"
    "v_exp_f32 v128, v128\n"
    "v_mfma_f32_16x16x32_bf16 v[136:139], a[108:111], a[44:47], v[136:139]\n"
    "v_cvt_pk_bf16_f32 v175, v158, v159\n"
    "v_mfma_f32_16x16x32_bf16 v[140:143], a[124:127], a[44:47], v[184:187]\n"
    "v_mfma_f32_16x16x32_bf16 v[140:143], a[120:123], a[40:43], v[140:143]\n"
    "global_load_lds_dwordx4 v201, s[40:41] offset:2048 sc1\n"
    "v_mfma_f32_16x16x32_bf16 v[140:143], a[116:119], a[36:39], v[140:143]\n"
    "v_exp_f32 v129, v129\n"
    "v_mfma_f32_16x16x32_bf16 v[140:143], a[112:115], a[32:35], v[140:143]\n"
    "v_exp_f32 v130, v130\n"
    "v_mfma_f32_16x16x32_bf16 v[32:35], v[168:171], a[128:131], v[32:35]\n"
    "v_exp_f32 v131, v131\n"
    "v_mfma_f32_16x16x32_bf16 v[32:35], v[172:175], a[132:135], v[32:35]\n"
    "v_cvt_pk_bf16_f32 v160, v128, v129\n"
    "v_mfma_f32_16x16x32_bf16 v[36:39], v[168:171], a[136:139], v[36:39]\n"
    "global_load_lds_dwordx4 v201, s[40:41] offset:3072 sc1\n"
    "v_mfma_f32_16x16x32_bf16 v[36:39], v[172:175], a[140:143], v[36:39]\n"
    "s_add_u32 m0, s6, 0x1000\n"
    "v_exp_f32 v132, v132\n"
    "v_mfma_f32_16x16x32_bf16 v[40:43], v[168:171], a[144:147], v[40:43]\n"
    "v_exp_f32 v133, v133\n"
    "v_mfma_f32_16x16x32_bf16 v[40:43], v[172:175], a[148:151], v[40:43]\n"
    "v_cvt_pk_bf16_f32 v161, v130, v131\n"
    "v_cvt_pk_bf16_f32 v162, v132, v133\n"
    "v_mfma_f32_16x16x32_bf16 v[44:47], v[168:171], a[152:155], v[44:47]\n"
    "v_mfma_f32_16x16x32_bf16 v[44:47], v[172:175], a[156:159], v[44:47]\n"
    "global_load_lds_dwordx4 v202, s[40:41] offset:0 sc1\n"
    "v_mfma_f32_16x16x32_bf16 v[48:51], v[168:171], a[160:163], v[48:51]\n"
    "v_exp_f32 v134, v134\n"
    "v_mfma_f32_16x16x32_bf16 v[48:51], v[172:175], a[164:167], v[48:51]\n"
    "v_exp_f32 v135, v135\n"
    "v_mfma_f32_16x16x32_bf16 v[52:55], v[168:171], a[168:171], v[52:55]\n"
    "v_exp_f32 v136, v136\n"
    "v_mfma_f32_16x16x32_bf16 v[52:55], v[172:175], a[172:175], v[52:55]\n"
    "v_cvt_pk_bf16_f32 v163, v134, v135\n"
    "v_mfma_f32_16x16x32_bf16 v[56:59], v[168:171], a[176:179], v[56:59]\n"
    "global_load_lds_dwordx4 v202, s[40:41] offset:1024 sc1\n"
    "v_mfma_f32_16x16x32_bf16 v[56:59], v[172:175], a[180:183], v[56:59]\n"
    "v_exp_f32 v137, v137\n"
    "v_mfma_f32_16x16x32_bf16 v[60:63], v[168:171], a[184:187], v[60:63]\n"
    "v_exp_f32 v138, v138\n"
    "v_mfma_f32_16x16x32_bf16 v[60:63], v[172:175], a[188:191], v[60:63]\n"
    "v_cvt_pk_bf16_f32 v164, v136, v137\n"
    "v_smfmac_f32_16x16x64_bf16 a[200:203], a[192:195], v[168:175], v240\n"
    "v_mfma_f32_16x16x32_bf16 v[144:147], a[64:67], a[48:51], v[188:191]\n"
    "v_exp_f32 v139, v139\n"
    "v_mfma_f32_16x16x32_bf16 v[144:147], a[68:71], a[52:55], v[144:147]\n"
    "v_exp_f32 v140, v140\n"
    "v_mfma_f32_16x16x32_bf16 v[144:147], a[72:75], a[56:59], v[144:147]\n"
    "ds_read_b128 a[64:67], v201 offset:32768\n"
    "v_mfma_f32_16x16x32_bf16 v[144:147], a[76:79], a[60:63], v[144:147]\n"
    "global_load_lds_dwordx4 v202, s[40:41] offset:2048 sc1\n"
    "v_mfma_f32_16x16x32_bf16 v[148:151], a[92:95], a[60:63], v[188:191]\n"
    "ds_read_b128 a[68:71], v201 offset:33792\n"
    "v_cvt_pk_bf16_f32 v165, v138, v139\n"
    "v_mfma_f32_16x16x32_bf16 v[148:151], a[88:91], a[56:59], v[148:151]\n"
    "v_exp_f32 v141, v141\n"
    "v_mfma_f32_16x16x32_bf16 v[148:151], a[84:87], a[52:55], v[148:151]\n"
    "ds_read_b128 a[72:75], v201 offset:34816\n"
    "v_cvt_pk_bf16_f32 v166, v140, v141\n"
    "v_mfma_f32_16x16x32_bf16 v[148:151], a[80:83], a[48:51], v[148:151]\n"
    "v_exp_f32 v142, v142\n"
    "v_mfma_f32_16x16x32_bf16 v[152:155], a[96:99], a[48:51], v[188:191]\n"
    "v_exp_f32 v143, v143\n"
    "v_mfma_f32_16x16x32_bf16 v[152:155], a[100:103], a[52:55], v[152:155]\n"
    "ds_read_b128 a[76:79], v201 offset:35840\n"
    "v_cvt_pk_bf16_f32 v167, v142, v143\n"
    "v_mfma_f32_16x16x32_bf16 v[152:155], a[104:107], a[56:59], v[152:155]\n"
    "ds_read_b128 a[92:95], v201 offset:39936\n"
    "v_mfma_f32_16x16x32_bf16 v[152:155], a[108:111], a[60:63], v[152:155]\n"
    "global_load_lds_dwordx4 v202, s[40:41] offset:3072 sc1\n"
    "v_mfma_f32_16x16x32_bf16 v[156:159], a[124:127], a[60:63], v[188:191]\n"
    "s_add_u32 s40, s40, 0x8000\n"
    "v_exp_f32 v144, v144\n"
    "v_mfma_f32_16x16x32_bf16 v[156:159], a[120:123], a[56:59], v[156:159]\n"
    "s_addc_u32 s41, s41, 0\n"
    "ds_read_b128 a[88:91], v201 offset:38912\n"
    "v_mfma_f32_16x16x32_bf16 v[156:159], a[116:119], a[52:55], v[156:159]\n"
    "v_exp_f32 v145, v145\n"
    "v_mfma_f32_16x16x32_bf16 v[156:159], a[112:115], a[48:51], v[156:159]\n"
    "ds_read_b128 a[84:87], v201 offset:37888\n"
    "v_cvt_pk_bf16_f32 v168, v144, v145\n"
    "v_mfma_f32_16x16x32_bf16 v[64:67], v[160:163], a[128:131], v[64:67]\n"
    "v_exp_f32 v146, v146\n"
    "v_mfma_f32_16x16x32_bf16 v[64:67], v[164:167], a[132:135], v[64:67]\n"
    "ds_read_b128 a[80:83], v201 offset:36864\n"
    "v_mfma_f32_16x16x32_bf16 v[68:71], v[160:163], a[136:139], v[68:71]\n"
    "v_exp_f32 v147, v147\n"
    "v_mfma_f32_16x16x32_bf16 v[68:71], v[164:167], a[140:143], v[68:71]\n"
    "v_exp_f32 v148, v148\n"
    "v_mfma_f32_16x16x32_bf16 v[72:75], v[160:163], a[144:147], v[72:75]\n"
    "ds_read_b128 a[96:99], v201 offset:40960\n"
    "v_cvt_pk_bf16_f32 v169, v146, v147\n"
    "v_mfma_f32_16x16x32_bf16 v[72:75], v[164:167], a[148:151], v[72:75]\n"
    "v_exp_f32 v149, v149\n"
    "v_mfma_f32_16x16x32_bf16 v[76:79], v[160:163], a[152:155], v[76:79]\n"
    "ds_read_b128 a[100:103], v201 offset:41984\n"
    "v_cvt_pk_bf16_f32 v170, v148, v149\n"
    "v_mfma_f32_16x16x32_bf16 v[76:79], v[164:167], a[156:159], v[76:79]\n"
    "v_exp_f32 v150, v150\n"
    "v_mfma_f32_16x16x32_bf16 v[80:83], v[160:163], a[160:163], v[80:83]\n"
    "ds_read_b128 a[104:107], v201 offset:43008\n"
    "v_mfma_f32_16x16x32_bf16 v[80:83], v[164:167], a[164:167], v[80:83]\n"
    "v_exp_f32 v151, v151\n"
    "v_mfma_f32_16x16x32_bf16 v[84:87], v[160:163], a[168:171], v[84:87]\n"
    "ds_read_b128 a[108:111], v201 offset:44032\n"
    "v_cvt_pk_bf16_f32 v171, v150, v151\n"
    "v_mfma_f32_16x16x32_bf16 v[84:87], v[164:167], a[172:175], v[84:87]\n"
    "v_exp_f32 v152, v152\n"
    "v_mfma_f32_16x16x32_bf16 v[88:91], v[160:163], a[176:179], v[88:91]\n"
    "ds_read_b128 a[124:127], v201 offset:48128\n"
    "v_mfma_f32_16x16x32_bf16 v[88:91], v[164:167], a[180:183], v[88:91]\n"
    "v_exp_f32 v153, v153\n"
    "v_mfma_f32_16x16x32_bf16 v[92:95], v[160:163], a[184:187], v[92:95]\n"
    "v_exp_f32 v154, v154\n"
    "v_mfma_f32_16x16x32_bf16 v[92:95], v[164:167], a[188:191], v[92:95]\n"
    "ds_read_b128 a[120:123], v201 offset:47104\n"
    "v_cvt_pk_bf16_f32 v172, v152, v153\n"
    "v_smfmac_f32_16x16x64_bf16 a[204:207], a[192:195], v[160:167], v240\n"
    "s_waitcnt lgkmcnt(10)\n"
    "v_mfma_f32_16x16x32_bf16 v[128:131], a[64:67], a[0:3], v[176:179]\n"
    "ds_read_b128 a[116:119], v201 offset:46080\n"
    "v_mfma_f32_16x16x32_bf16 v[128:131], a[68:71], a[4:7], v[128:131]\n"
    "v_exp_f32 v155, v155\n"
    "v_mfma_f32_16x16x32_bf16 v[128:131], a[72:75], a[8:11], v[128:131]\n"
    "ds_read_b128 a[112:115], v201 offset:45056\n"
    "v_cvt_pk_bf16_f32 v173, v154, v155\n"
    "v_mfma_f32_16x16x32_bf16 v[128:131], a[76:79], a[12:15], v[128:131]\n"
    "s_waitcnt lgkmcnt(8)\n"
    "v_exp_f32 v156, v156\n"
    "v_mfma_f32_16x16x32_bf16 v[132:135], a[92:95], a[12:15], v[176:179]\n"
    "v_exp_f32 v157, v157\n"
    "v_mfma_f32_16x16x32_bf16 v[132:135], a[88:91], a[8:11], v[132:135]\n"
    "v_exp_f32 v158, v158\n"
    "v_mfma_f32_16x16x32_bf16 v[132:135], a[84:87], a[4:7], v[132:135]\n"
    "v_cvt_pk_bf16_f32 v174, v156, v157\n"
    "v_mfma_f32_16x16x32_bf16 v[132:135], a[80:83], a[0:3], v[132:135]\n"
    "s_waitcnt lgkmcnt(4)\n"
    "v_exp_f32 v159, v159\n"
    "v_mfma_f32_16x16x32_bf16 v[136:139], a[96:99], a[0:3], v[176:179]\n"
    "v_cvt_pk_bf16_f32 v175, v158, v159\n"
    "v_mfma_f32_16x16x32_bf16 v[136:139], a[100:103], a[4:7], v[136:139]\n"
    "v_mfma_f32_16x16x32_bf16 v[136:139], a[104:107], a[8:11], v[136:139]\n"
    "v_exp_f32 v128, v128\n"
    "v_mfma_f32_16x16x32_bf16 v[136:139], a[108:111], a[12:15], v[136:139]\n"
    "s_waitcnt lgkmcnt(0)\n"
    "v_mfma_f32_16x16x32_bf16 v[140:143], a[124:127], a[12:15], v[176:179]\n"
    "v_exp_f32 v129, v129\n"
    "v_mfma_f32_16x16x32_bf16 v[140:143], a[120:123], a[8:11], v[140:143]\n"
    "v_cvt_pk_bf16_f32 v160, v128, v129\n"
    "v_mfma_f32_16x16x32_bf16 v[140:143], a[116:119], a[4:7], v[140:143]\n"
    "v_exp_f32 v130, v130\n"
    "v_mfma_f32_16x16x32_bf16 v[140:143], a[112:115], a[0:3], v[140:143]\n"
    "v_mfma_f32_16x16x32_bf16 v[96:99], v[168:171], a[128:131], v[96:99]\n"
    "v_exp_f32 v131, v131\n"
    "v_mfma_f32_16x16x32_bf16 v[96:99], v[172:175], a[132:135], v[96:99]\n"
    "ds_read_b128 a[128:131], v201 offset:49152\n"
    "v_cvt_pk_bf16_f32 v161, v130, v131\n"
    "v_mfma_f32_16x16x32_bf16 v[100:103], v[168:171], a[136:139], v[100:103]\n"
    "205: s_cbranch_execz 199b\n"
    "v_exp_f32 v132, v132\n"
    "v_mfma_f32_16x16x32_bf16 v[100:103], v[172:175], a[140:143], v[100:103]\n"
    "ds_read_b128 a[132:135], v201 offset:57344\n"
    "v_mfma_f32_16x16x32_bf16 v[104:107], v[168:171], a[144:147], v[104:107]\n"
    "v_exp_f32 v133, v133\n"
    "v_mfma_f32_16x16x32_bf16 v[104:107], v[172:175], a[148:151], v[104:107]\n"
    "ds_read_b128 a[136:139], v201 offset:50176\n"
    "v_cvt_pk_bf16_f32 v162, v132, v133\n"
    "v_mfma_f32_16x16x32_bf16 v[108:111], v[168:171], a[152:155], v[108:111]\n"
    "v_exp_f32 v134, v134\n"
    "v_mfma_f32_16x16x32_bf16 v[108:111], v[172:175], a[156:159], v[108:111]\n"
    "v_mfma_f32_16x16x32_bf16 v[112:115], v[168:171], a[160:163], v[112:115]\n"
    "v_exp_f32 v135, v135\n"
    "v_mfma_f32_16x16x32_bf16 v[112:115], v[172:175], a[164:167], v[112:115]\n"
    "ds_read_b128 a[140:143], v201 offset:58368\n"
    "v_cvt_pk_bf16_f32 v163, v134, v135\n"
    "v_mfma_f32_16x16x32_bf16 v[116:119], v[168:171], a[168:171], v[116:119]\n"
    "v_exp_f32 v136, v136\n"
    "v_mfma_f32_16x16x32_bf16 v[116:119], v[172:175], a[172:175], v[116:119]\n"
    "ds_read_b128 a[144:147], v201 offset:51200\n"
    "v_mfma_f32_16x16x32_bf16 v[120:123], v[168:171], a[176:179], v[120:123]\n"
    "v_exp_f32 v137, v137\n"
    "v_mfma_f32_16x16x32_bf16 v[120:123], v[172:175], a[180:183], v[120:123]\n"
    "ds_read_b128 a[148:151], v201 offset:59392\n"
    "v_cvt_pk_bf16_f32 v164, v136, v137\n"
    "v_mfma_f32_16x16x32_bf16 v[124:127], v[168:171], a[184:187], v[124:127]\n"
    "v_exp_f32 v138, v138\n"
    "v_mfma_f32_16x16x32_bf16 v[124:127], v[172:175], a[188:191], v[124:127]\n"
    "ds_read_b128 a[152:155], v201 offset:52224\n"
    "v_smfmac_f32_16x16x64_bf16 a[208:211], a[192:195], v[168:175], v240\n"
    "ds_read_b128 a[156:159], v201 offset:60416\n"
    "v_mfma_f32_16x16x32_bf16 v[144:147], a[64:67], a[16:19], v[180:183]\n"
    "v_exp_f32 v139, v139\n"
    "v_mfma_f32_16x16x32_bf16 v[144:147], a[68:71], a[20:23], v[144:147]\n"
    "v_exp_f32 v140, v140\n"
    "v_mfma_f32_16x16x32_bf16 v[144:147], a[72:75], a[24:27], v[144:147]\n"
    "ds_read_b128 a[160:163], v201 offset:53248\n"
    "v_cvt_pk_bf16_f32 v165, v138, v139\n"
    "v_mfma_f32_16x16x32_bf16 v[144:147], a[76:79], a[28:31], v[144:147]\n"
    "v_exp_f32 v141, v141\n"
    "v_mfma_f32_16x16x32_bf16 v[148:151], a[92:95], a[28:31], v[180:183]\n"
    "ds_read_b128 a[164:167], v201 offset:61440\n"
    "v_cvt_pk_bf16_f32 v166, v140, v141\n"
    "v_mfma_f32_16x16x32_bf16 v[148:151], a[88:91], a[24:27], v[148:151]\n"
    "v_exp_f32 v142, v142\n"
    "v_mfma_f32_16x16x32_bf16 v[148:151], a[84:87], a[20:23], v[148:151]\n"
    "ds_read_b128 a[168:171], v201 offset:54272\n"
    "v_mfma_f32_16x16x32_bf16 v[148:151], a[80:83], a[16:19], v[148:151]\n"
    "v_exp_f32 v143, v143\n"
    "v_mfma_f32_16x16x32_bf16 v[152:155], a[96:99], a[16:19], v[180:183]\n"
    "ds_read_b128 a[172:175], v201 offset:62464\n"
    "v_cvt_pk_bf16_f32 v167, v142, v143\n"
    "v_mfma_f32_16x16x32_bf16 v[152:155], a[100:103], a[20:23], v[152:155]\n"
    "v_mfma_f32_16x16x32_bf16 v[152:155], a[104:107], a[24:27], v[152:155]\n"
    "v_exp_f32 v144, v144\n"
    "v_mfma_f32_16x16x32_bf16 v[152:155], a[108:111], a[28:31], v[152:155]\n"
    "ds_read_b128 a[176:179], v201 offset:55296\n"
    "v_mfma_f32_16x16x32_bf16 v[156:159], a[124:127], a[28:31], v[180:183]\n"
    "v_exp_f32 v145, v145\n"
    "v_mfma_f32_16x16x32_bf16 v[156:159], a[120:123], a[24:27], v[156:159]\n"
    "ds_read_b128 a[180:183], v201 offset:63488\n"
    "v_cvt_pk_bf16_f32 v168, v144, v145\n"
    "v_mfma_f32_16x16x32_bf16 v[156:159], a[116:119], a[20:23], v[156:159]\n"
    "v_exp_f32 v146, v146\n"
    "v_mfma_f32_16x16x32_bf16 v[156:159], a[112:115], a[16:19], v[156:159]\n"
    "s_waitcnt lgkmcnt(10)\n"
    "ds_read_b128 a[184:187], v201 offset:56320\n"
    "v_mfma_f32_16x16x32_bf16 v[0:3], v[160:163], a[128:131], v[0:3]\n"
    "v_exp_f32 v147, v147\n"
    "v_mfma_f32_16x16x32_bf16 v[0:3], v[164:167], a[132:135], v[0:3]\n"
    "ds_read_b128 a[188:191], v201 offset:64512\n"
    "v_cvt_pk_bf16_f32 v169, v146, v147\n"
    "v_mfma_f32_16x16x32_bf16 v[4:7], v[160:163], a[136:139], v[4:7]\n"
    "v_exp_f32 v148, v148\n"
    "v_mfma_f32_16x16x32_bf16 v[4:7], v[164:167], a[140:143], v[4:7]\n"
    "s_waitcnt lgkmcnt(8)\n"
    "v_mfma_f32_16x16x32_bf16 v[8:11], v[160:163], a[144:147], v[8:11]\n"
    "v_exp_f32 v149, v149\n"
    "v_mfma_f32_16x16x32_bf16 v[8:11], v[164:167], a[148:151], v[8:11]\n"
    "v_cvt_pk_bf16_f32 v170, v148, v149\n"
    "v_mfma_f32_16x16x32_bf16 v[12:15], v[160:163], a[152:155], v[12:15]\n"
    "v_exp_f32 v150, v150\n"
    "v_mfma_f32_16x16x32_bf16 v[12:15], v[164:167], a[156:159], v[12:15]\n"
    "s_waitcnt lgkmcnt(4)\n"
    "v_mfma_f32_16x16x32_bf16 v[16:19], v[160:163], a[160:163], v[16:19]\n"
    "v_exp_f32 v151, v151\n"
    "v_mfma_f32_16x16x32_bf16 v[16:19], v[164:167], a[164:167], v[16:19]\n"
    "v_cvt_pk_bf16_f32 v171, v150, v151\n"
    "v_mfma_f32_16x16x32_bf16 v[20:23], v[160:163], a[168:171], v[20:23]\n"
    "v_exp_f32 v152, v152\n"
    "v_mfma_f32_16x16x32_bf16 v[20:23], v[164:167], a[172:175], v[20:23]\n"
    "s_waitcnt lgkmcnt(0)\n"
    "v_mfma_f32_16x16x32_bf16 v[24:27], v[160:163], a[176:179], v[24:27]\n"
    "s_add_u32 m0, s6, 0x8000\n"
    "v_exp_f32 v153, v153\n"
    "v_mfma_f32_16x16x32_bf16 v[24:27], v[164:167], a[180:183], v[24:27]\n"
    "v_cvt_pk_bf16_f32 v172, v152, v153\n"
    "v_mfma_f32_16x16x32_bf16 v[28:31], v[160:163], a[184:187], v[28:31]\n"
    "v_exp_f32 v154, v154\n"
    "v_mfma_f32_16x16x32_bf16 v[28:31], v[164:167], a[188:191], v[28:31]\n"
    "v_smfmac_f32_16x16x64_bf16 a[196:199], a[192:195], v[160:167], v240\n"
    "v_mfma_f32_16x16x32_bf16 v[128:131], a[64:67], a[32:35], v[184:187]\n"
    "v_exp_f32 v155, v155\n"
    "v_mfma_f32_16x16x32_bf16 v[128:131], a[68:71], a[36:39], v[128:131]\n"
    "s_waitcnt vmcnt(8)\n"
    "v_cvt_pk_bf16_f32 v173, v154, v155\n"
    "v_mfma_f32_16x16x32_bf16 v[128:131], a[72:75], a[40:43], v[128:131]\n"
    "s_barrier\n"
    "v_mfma_f32_16x16x32_bf16 v[128:131], a[76:79], a[44:47], v[128:131]\n"
    "global_load_lds_dwordx4 v201, s[40:41] offset:0 sc1\n"
    "v_mfma_f32_16x16x32_bf16 v[132:135], a[92:95], a[44:47], v[184:187]\n"
    "v_exp_f32 v156, v156\n"
    "v_mfma_f32_16x16x32_bf16 v[132:135], a[88:91], a[40:43], v[132:135]\n"
    "v_exp_f32 v157, v157\n"
    "v_mfma_f32_16x16x32_bf16 v[132:135], a[84:87], a[36:39], v[132:135]\n"
    "v_exp_f32 v158, v158\n"
    "v_mfma_f32_16x16x32_bf16 v[132:135], a[80:83], a[32:35], v[132:135]\n"
    "v_cvt_pk_bf16_f32 v174, v156, v157\n"
    "v_mfma_f32_16x16x32_bf16 v[136:139], a[96:99], a[32:35], v[184:187]\n"
    "global_load_lds_dwordx4 v201, s[40:41] offset:1024 sc1\n"
    "v_mfma_f32_16x16x32_bf16 v[136:139], a[100:103], a[36:39], v[136:139]\n"
    "v_exp_f32 v159, v159\n"
    "v_mfma_f32_16x16x32_bf16 v[136:139], a[104:107], a[40:43], v[136:139]\n"
    "v_exp_f32 v128, v128\n"
    "v_mfma_f32_16x16x32_bf16 v[136:139], a[108:111], a[44:47], v[136:139]\n"
    "v_cvt_pk_bf16_f32 v175, v158, v159\n"
    "v_mfma_f32_16x16x32_bf16 v[140:143], a[124:127], a[44:47], v[184:187]\n"
    "v_mfma_f32_16x16x32_bf16 v[140:143], a[120:123], a[40:43], v[140:143]\n"
    "global_load_lds_dwordx4 v201, s[40:41] offset:2048 sc1\n"
    "v_mfma_f32_16x16x32_bf16 v[140:143], a[116:119], a[36:39], v[140:143]\n"
    "v_exp_f32 v129, v129\n"
    "v_mfma_f32_16x16x32_bf16 v[140:143], a[112:115], a[32:35], v[140:143]\n"
    "v_exp_f32 v130, v130\n"
    "v_mfma_f32_16x16x32_bf16 v[32:35], v[168:171], a[128:131], v[32:35]\n"
    "v_exp_f32 v131, v131\n"
    "v_mfma_f32_16x16x32_bf16 v[32:35], v[172:175], a[132:135], v[32:35]\n"
    "v_cvt_pk_bf16_f32 v160, v128, v129\n"
    "v_mfma_f32_16x16x32_bf16 v[36:39], v[168:171], a[136:139], v[36:39]\n"
    "global_load_lds_dwordx4 v201, s[40:41] offset:3072 sc1\n"
    "v_mfma_f32_16x16x32_bf16 v[36:39], v[172:175], a[140:143], v[36:39]\n"
    "s_add_u32 m0, s6, 0x9000\n"
    "v_exp_f32 v132, v132\n"
    "v_mfma_f32_16x16x32_bf16 v[40:43], v[168:171], a[144:147], v[40:43]\n"
    "v_exp_f32 v133, v133\n"
    "v_mfma_f32_16x16x32_bf16 v[40:43], v[172:175], a[148:151], v[40:43]\n"
    "v_cvt_pk_bf16_f32 v161, v130, v131\n"
    "v_cvt_pk_bf16_f32 v162, v132, v133\n"
    "v_mfma_f32_16x16x32_bf16 v[44:47], v[168:171], a[152:155], v[44:47]\n"
    "v_mfma_f32_16x16x32_bf16 v[44:47], v[172:175], a[156:159], v[44:47]\n"
    "global_load_lds_dwordx4 v202, s[40:41] offset:0 sc1\n"
    "v_mfma_f32_16x16x32_bf16 v[48:51], v[168:171], a[160:163], v[48:51]\n"
    "v_exp_f32 v134, v134\n"
    "v_mfma_f32_16x16x32_bf16 v[48:51], v[172:175], a[164:167], v[48:51]\n"
    "v_exp_f32 v135, v135\n"
    "v_mfma_f32_16x16x32_bf16 v[52:55], v[168:171], a[168:171], v[52:55]\n"
    "v_exp_f32 v136, v136\n"
    "v_mfma_f32_16x16x32_bf16 v[52:55], v[172:175], a[172:175], v[52:55]\n"
    "v_cvt_pk_bf16_f32 v163, v134, v135\n"
    "v_mfma_f32_16x16x32_bf16 v[56:59], v[168:171], a[176:179], v[56:59]\n"
    "global_load_lds_dwordx4 v202, s[40:41] offset:1024 sc1\n"
    "v_mfma_f32_16x16x32_bf16 v[56:59], v[172:175], a[180:183], v[56:59]\n"
    "v_exp_f32 v137, v137\n"
    "v_mfma_f32_16x16x32_bf16 v[60:63], v[168:171], a[184:187], v[60:63]\n"
    "v_exp_f32 v138, v138\n"
    "v_mfma_f32_16x16x32_bf16 v[60:63], v[172:175], a[188:191], v[60:63]\n"
    "v_cvt_pk_bf16_f32 v164, v136, v137\n"
    "v_smfmac_f32_16x16x64_bf16 a[200:203], a[192:195], v[168:175], v240\n"
    "v_mfma_f32_16x16x32_bf16 v[144:147], a[64:67], a[48:51], v[188:191]\n"
    "v_exp_f32 v139, v139\n"
    "v_mfma_f32_16x16x32_bf16 v[144:147], a[68:71], a[52:55], v[144:147]\n"
    "v_exp_f32 v140, v140\n"
    "v_mfma_f32_16x16x32_bf16 v[144:147], a[72:75], a[56:59], v[144:147]\n"
    "ds_read_b128 a[64:67], v203 offset:0\n"
    "v_mfma_f32_16x16x32_bf16 v[144:147], a[76:79], a[60:63], v[144:147]\n"
    "global_load_lds_dwordx4 v202, s[40:41] offset:2048 sc1\n"
    "v_mfma_f32_16x16x32_bf16 v[148:151], a[92:95], a[60:63], v[188:191]\n"
    "ds_read_b128 a[68:71], v203 offset:1024\n"
    "v_cvt_pk_bf16_f32 v165, v138, v139\n"
    "v_mfma_f32_16x16x32_bf16 v[148:151], a[88:91], a[56:59], v[148:151]\n"
    "v_exp_f32 v141, v141\n"
    "v_mfma_f32_16x16x32_bf16 v[148:151], a[84:87], a[52:55], v[148:151]\n"
    "ds_read_b128 a[72:75], v203 offset:2048\n"
    "v_cvt_pk_bf16_f32 v166, v140, v141\n"
    "v_mfma_f32_16x16x32_bf16 v[148:151], a[80:83], a[48:51], v[148:151]\n"
    "v_exp_f32 v142, v142\n"
    "v_mfma_f32_16x16x32_bf16 v[152:155], a[96:99], a[48:51], v[188:191]\n"
    "v_exp_f32 v143, v143\n"
    "v_mfma_f32_16x16x32_bf16 v[152:155], a[100:103], a[52:55], v[152:155]\n"
    "ds_read_b128 a[76:79], v203 offset:3072\n"
    "v_cvt_pk_bf16_f32 v167, v142, v143\n"
    "v_mfma_f32_16x16x32_bf16 v[152:155], a[104:107], a[56:59], v[152:155]\n"
    "ds_read_b128 a[92:95], v203 offset:7168\n"
    "v_mfma_f32_16x16x32_bf16 v[152:155], a[108:111], a[60:63], v[152:155]\n"
    "global_load_lds_dwordx4 v202, s[40:41] offset:3072 sc1\n"
    "v_mfma_f32_16x16x32_bf16 v[156:159], a[124:127], a[60:63], v[188:191]\n"
    "s_add_u32 s40, s40, 0x8000\n"
    "v_exp_f32 v144, v144\n"
    "v_mfma_f32_16x16x32_bf16 v[156:159], a[120:123], a[56:59], v[156:159]\n"
    "s_addc_u32 s41, s41, 0\n"
    "ds_read_b128 a[88:91], v203 offset:6144\n"
    "v_mfma_f32_16x16x32_bf16 v[156:159], a[116:119], a[52:55], v[156:159]\n"
    "v_exp_f32 v145, v145\n"
    "v_mfma_f32_16x16x32_bf16 v[156:159], a[112:115], a[48:51], v[156:159]\n"
    "ds_read_b128 a[84:87], v203 offset:5120\n"
    "v_cvt_pk_bf16_f32 v168, v144, v145\n"
    "v_mfma_f32_16x16x32_bf16 v[64:67], v[160:163], a[128:131], v[64:67]\n"
    "v_exp_f32 v146, v146\n"
    "v_mfma_f32_16x16x32_bf16 v[64:67], v[164:167], a[132:135], v[64:67]\n"
    "ds_read_b128 a[80:83], v203 offset:4096\n"
    "v_mfma_f32_16x16x32_bf16 v[68:71], v[160:163], a[136:139], v[68:71]\n"
    "v_exp_f32 v147, v147\n"
    "v_mfma_f32_16x16x32_bf16 v[68:71], v[164:167], a[140:143], v[68:71]\n"
    "v_exp_f32 v148, v148\n"
    "v_mfma_f32_16x16x32_bf16 v[72:75], v[160:163], a[144:147], v[72:75]\n"
    "ds_read_b128 a[96:99], v203 offset:8192\n"
    "v_cvt_pk_bf16_f32 v169, v146, v147\n"
    "v_mfma_f32_16x16x32_bf16 v[72:75], v[164:167], a[148:151], v[72:75]\n"
    "v_exp_f32 v149, v149\n"
    "v_mfma_f32_16x16x32_bf16 v[76:79], v[160:163], a[152:155], v[76:79]\n"
    "ds_read_b128 a[100:103], v203 offset:9216\n"
    "v_cvt_pk_bf16_f32 v170, v148, v149\n"
    "v_mfma_f32_16x16x32_bf16 v[76:79], v[164:167], a[156:159], v[76:79]\n"
    "v_exp_f32 v150, v150\n"
    "v_mfma_f32_16x16x32_bf16 v[80:83], v[160:163], a[160:163], v[80:83]\n"
    "ds_read_b128 a[104:107], v203 offset:10240\n"
    "v_mfma_f32_16x16x32_bf16 v[80:83], v[164:167], a[164:167], v[80:83]\n"
    "s_sub_u32 s51, s51, 1\n"
    "v_exp_f32 v151, v151\n"
    "v_mfma_f32_16x16x32_bf16 v[84:87], v[160:163], a[168:171], v[84:87]\n"
    "ds_read_b128 a[108:111], v203 offset:11264\n"
    "v_cvt_pk_bf16_f32 v171, v150, v151\n"
    "v_mfma_f32_16x16x32_bf16 v[84:87], v[164:167], a[172:175], v[84:87]\n"
    "v_exp_f32 v152, v152\n"
    "v_mfma_f32_16x16x32_bf16 v[88:91], v[160:163], a[176:179], v[88:91]\n"
    "ds_read_b128 a[124:127], v203 offset:15360\n"
    "v_mfma_f32_16x16x32_bf16 v[88:91], v[164:167], a[180:183], v[88:91]\n"
    "v_exp_f32 v153, v153\n"
    "v_mfma_f32_16x16x32_bf16 v[92:95], v[160:163], a[184:187], v[92:95]\n"
    "v_exp_f32 v154, v154\n"
    "v_mfma_f32_16x16x32_bf16 v[92:95], v[164:167], a[188:191], v[92:95]\n"
    "ds_read_b128 a[120:123], v203 offset:14336\n"
    "v_cvt_pk_bf16_f32 v172, v152, v153\n"
    "v_smfmac_f32_16x16x64_bf16 a[204:207], a[192:195], v[160:167], v240\n"
    "s_waitcnt lgkmcnt(10)\n"
    "s_cbranch_scc0 1b\n"
    "v_mfma_f32_16x16x32_bf16 v[128:131], a[64:67], a[0:3], v[176:179]\n"
    "v_exp_f32 v155, v155\n"
    "v_mfma_f32_16x16x32_bf16 v[128:131], a[68:71], a[4:7], v[128:131]\n"
    "v_exp_f32 v156, v156\n"
    "v_mfma_f32_16x16x32_bf16 v[128:131], a[72:75], a[8:11], v[128:131]\n"
    "v_exp_f32 v157, v157\n"
    "v_mfma_f32_16x16x32_bf16 v[128:131], a[76:79], a[12:15], v[128:131]\n"
    "s_waitcnt lgkmcnt(0)\n"
    "v_exp_f32 v158, v158\n"
    "v_mfma_f32_16x16x32_bf16 v[132:135], a[92:95], a[12:15], v[176:179]\n"
    "v_exp_f32 v159, v159\n"
    "v_mfma_f32_16x16x32_bf16 v[132:135], a[88:91], a[8:11], v[132:135]\n"
    "ds_read_b128 a[116:119], v203 offset:13312\n"
    "v_cvt_pk_bf16_f32 v173, v154, v155\n"
    "v_mfma_f32_16x16x32_bf16 v[132:135], a[84:87], a[4:7], v[132:135]\n"
    "ds_read_b128 a[112:115], v203 offset:12288\n"
    "v_cvt_pk_bf16_f32 v174, v156, v157\n"
    "v_mfma_f32_16x16x32_bf16 v[132:135], a[80:83], a[0:3], v[132:135]\n"
    "v_cvt_pk_bf16_f32 v175, v158, v159\n"
    "v_mov_b32 v228, 1.0\n"
    "v_mfma_f32_16x16x32_bf16 v[136:139], a[96:99], a[0:3], v[176:179]\n"
    "v_mov_b32 v252, 0x1b000\n"
    "v_mfma_f32_16x16x32_bf16 v[136:139], a[100:103], a[4:7], v[136:139]\n"
    "v_mfma_f32_16x16x32_bf16 v[136:139], a[104:107], a[8:11], v[136:139]\n"
    "v_exp_f32 v128, v128\n"
    "v_mfma_f32_16x16x32_bf16 v[136:139], a[108:111], a[12:15], v[136:139]\n"
    "v_exp_f32 v129, v129\n"
    "v_mfma_f32_16x16x32_bf16 v[140:143], a[124:127], a[12:15], v[176:179]\n"
    "v_cvt_pk_bf16_f32 v160, v128, v129\n"
    "v_mfma_f32_16x16x32_bf16 v[140:143], a[120:123], a[8:11], v[140:143]\n"
    "s_waitcnt lgkmcnt(1)\n"
    "v_exp_f32 v130, v130\n"
    "v_mfma_f32_16x16x32_bf16 v[140:143], a[116:119], a[4:7], v[140:143]\n"
    "s_waitcnt lgkmcnt(0)\n"
    "v_exp_f32 v131, v131\n"
    "v_mfma_f32_16x16x32_bf16 v[140:143], a[112:115], a[0:3], v[140:143]\n"
    "v_cvt_pk_bf16_f32 v161, v130, v131\n"
    "v_mfma_f32_16x16x32_bf16 v[96:99], v[168:171], a[128:131], v[96:99]\n"
    "v_exp_f32 v132, v132\n"
    "v_mfma_f32_16x16x32_bf16 v[96:99], v[172:175], a[132:135], v[96:99]\n"
    "ds_read_b128 a[128:131], v203 offset:16384\n"
    "v_mfma_f32_16x16x32_bf16 v[100:103], v[168:171], a[136:139], v[100:103]\n"
    "v_exp_f32 v133, v133\n"
    "v_mfma_f32_16x16x32_bf16 v[100:103], v[172:175], a[140:143], v[100:103]\n"
    "ds_read_b128 a[132:135], v203 offset:24576\n"
    "v_cvt_pk_bf16_f32 v162, v132, v133\n"
    "v_mfma_f32_16x16x32_bf16 v[104:107], v[168:171], a[144:147], v[104:107]\n"
    "v_exp_f32 v134, v134\n"
    "v_mfma_f32_16x16x32_bf16 v[104:107], v[172:175], a[148:151], v[104:107]\n"
    "ds_read_b128 a[136:139], v203 offset:17408\n"
    "v_mfma_f32_16x16x32_bf16 v[108:111], v[168:171], a[152:155], v[108:111]\n"
    "v_exp_f32 v135, v135\n"
    "v_mfma_f32_16x16x32_bf16 v[108:111], v[172:175], a[156:159], v[108:111]\n"
    "v_cvt_pk_bf16_f32 v163, v134, v135\n"
    "v_mfma_f32_16x16x32_bf16 v[112:115], v[168:171], a[160:163], v[112:115]\n"
    "ds_read_b128 a[140:143], v203 offset:25600\n"
    "v_mfma_f32_16x16x32_bf16 v[112:115], v[172:175], a[164:167], v[112:115]\n"
    "v_exp_f32 v136, v136\n"
    "v_mfma_f32_16x16x32_bf16 v[116:119], v[168:171], a[168:171], v[116:119]\n"
    "ds_read_b128 a[144:147], v203 offset:18432\n"
    "v_mfma_f32_16x16x32_bf16 v[116:119], v[172:175], a[172:175], v[116:119]\n"
    "v_exp_f32 v137, v137\n"
    "v_mfma_f32_16x16x32_bf16 v[120:123], v[168:171], a[176:179], v[120:123]\n"
    "ds_read_b128 a[148:151], v203 offset:26624\n"
    "v_cvt_pk_bf16_f32 v164, v136, v137\n"
    "v_mfma_f32_16x16x32_bf16 v[120:123], v[172:175], a[180:183], v[120:123]\n"
    "v_exp_f32 v138, v138\n"
    "v_mfma_f32_16x16x32_bf16 v[124:127], v[168:171], a[184:187], v[124:127]\n"
    "ds_read_b128 a[152:155], v203 offset:19456\n"
    "v_mfma_f32_16x16x32_bf16 v[124:127], v[172:175], a[188:191], v[124:127]\n"
    "v_exp_f32 v139, v139\n"
    "v_smfmac_f32_16x16x64_bf16 a[208:211], a[192:195], v[168:175], v240\n"
    "ds_read_b128 a[156:159], v203 offset:27648\n"
    "v_mfma_f32_16x16x32_bf16 v[144:147], a[64:67], a[16:19], v[180:183]\n"
    "v_cvt_pk_bf16_f32 v165, v138, v139\n"
    "v_mfma_f32_16x16x32_bf16 v[144:147], a[68:71], a[20:23], v[144:147]\n"
    "v_exp_f32 v140, v140\n"
    "v_mfma_f32_16x16x32_bf16 v[144:147], a[72:75], a[24:27], v[144:147]\n"
    "ds_read_b128 a[160:163], v203 offset:20480\n"
    "v_mfma_f32_16x16x32_bf16 v[144:147], a[76:79], a[28:31], v[144:147]\n"
    "v_exp_f32 v141, v141\n"
    "v_mfma_f32_16x16x32_bf16 v[148:151], a[92:95], a[28:31], v[180:183]\n"
    "ds_read_b128 a[164:167], v203 offset:28672\n"
    "v_cvt_pk_bf16_f32 v166, v140, v141\n"
    "v_mfma_f32_16x16x32_bf16 v[148:151], a[88:91], a[24:27], v[148:151]\n"
    "v_exp_f32 v142, v142\n"
    "v_mfma_f32_16x16x32_bf16 v[148:151], a[84:87], a[20:23], v[148:151]\n"
    "ds_read_b128 a[168:171], v203 offset:21504\n"
    "v_mfma_f32_16x16x32_bf16 v[148:151], a[80:83], a[16:19], v[148:151]\n"
    "v_exp_f32 v143, v143\n"
    "v_mfma_f32_16x16x32_bf16 v[152:155], a[96:99], a[16:19], v[180:183]\n"
    "ds_read_b128 a[172:175], v203 offset:29696\n"
    "v_cvt_pk_bf16_f32 v167, v142, v143\n"
    "v_mfma_f32_16x16x32_bf16 v[152:155], a[100:103], a[20:23], v[152:155]\n"
    "v_mfma_f32_16x16x32_bf16 v[152:155], a[104:107], a[24:27], v[152:155]\n"
    "v_exp_f32 v144, v144\n"
    "v_mfma_f32_16x16x32_bf16 v[152:155], a[108:111], a[28:31], v[152:155]\n"
    "ds_read_b128 a[176:179], v203 offset:22528\n"
    "v_mfma_f32_16x16x32_bf16 v[156:159], a[124:127], a[28:31], v[180:183]\n"
    "v_exp_f32 v145, v145\n"
    "v_mfma_f32_16x16x32_bf16 v[156:159], a[120:123], a[24:27], v[156:159]\n"
    "ds_read_b128 a[180:183], v203 offset:30720\n"
    "v_cvt_pk_bf16_f32 v168, v144, v145\n"
    "v_mfma_f32_16x16x32_bf16 v[156:159], a[116:119], a[20:23], v[156:159]\n"
    "v_exp_f32 v146, v146\n"
    "v_mfma_f32_16x16x32_bf16 v[156:159], a[112:115], a[16:19], v[156:159]\n"
    "s_waitcnt lgkmcnt(3)\n"
    "ds_read_b128 a[184:187], v203 offset:23552\n"
    "v_mfma_f32_16x16x32_bf16 v[0:3], v[160:163], a[128:131], v[0:3]\n"
    "v_exp_f32 v147, v147\n"
    "v_mfma_f32_16x16x32_bf16 v[0:3], v[164:167], a[132:135], v[0:3]\n"
    "ds_read_b128 a[188:191], v203 offset:31744\n"
    "v_cvt_pk_bf16_f32 v169, v146, v147\n"
    "v_mfma_f32_16x16x32_bf16 v[4:7], v[160:163], a[136:139], v[4:7]\n"
    "v_exp_f32 v148, v148\n"
    "v_mfma_f32_16x16x32_bf16 v[4:7], v[164:167], a[140:143], v[4:7]\n"
    "v_exp_f32 v149, v149\n"
    "v_mfma_f32_16x16x32_bf16 v[8:11], v[160:163], a[144:147], v[8:11]\n"
    "v_cvt_pk_bf16_f32 v170, v148, v149\n"
    "v_mfma_f32_16x16x32_bf16 v[8:11], v[164:167], a[148:151], v[8:11]\n"
    "v_exp_f32 v150, v150\n"
    "v_mfma_f32_16x16x32_bf16 v[12:15], v[160:163], a[152:155], v[12:15]\n"
    "v_exp_f32 v151, v151\n"
    "v_mfma_f32_16x16x32_bf16 v[12:15], v[164:167], a[156:159], v[12:15]\n"
    "v_cvt_pk_bf16_f32 v171, v150, v151\n"
    "v_mfma_f32_16x16x32_bf16 v[16:19], v[160:163], a[160:163], v[16:19]\n"
    "v_exp_f32 v152, v152\n"
    "v_mfma_f32_16x16x32_bf16 v[16:19], v[164:167], a[164:167], v[16:19]\n"
    "v_exp_f32 v153, v153\n"
    "v_mfma_f32_16x16x32_bf16 v[20:23], v[160:163], a[168:171], v[20:23]\n"
    "s_waitcnt lgkmcnt(0)\n"
    "v_cvt_pk_bf16_f32 v172, v152, v153\n"
    "v_mfma_f32_16x16x32_bf16 v[20:23], v[164:167], a[172:175], v[20:23]\n"
    "v_exp_f32 v154, v154\n"
    "v_mfma_f32_16x16x32_bf16 v[24:27], v[160:163], a[176:179], v[24:27]\n"
    "v_exp_f32 v155, v155\n"
    "v_mfma_f32_16x16x32_bf16 v[24:27], v[164:167], a[180:183], v[24:27]\n"
    "v_cvt_pk_bf16_f32 v173, v154, v155\n"
    "v_mfma_f32_16x16x32_bf16 v[28:31], v[160:163], a[184:187], v[28:31]\n"
    "s_waitcnt vmcnt(0)\n"
    "v_exp_f32 v156, v156\n"
    "v_mfma_f32_16x16x32_bf16 v[28:31], v[164:167], a[188:191], v[28:31]\n"
    "s_barrier\n"
    "v_smfmac_f32_16x16x64_bf16 a[196:199], a[192:195], v[160:167], v240\n"
    "ds_read_b128 v[232:235], v252\n"
    "v_mfma_f32_16x16x32_bf16 v[128:131], a[64:67], a[32:35], v[184:187]\n"
    "v_exp_f32 v157, v157\n"
    "v_mfma_f32_16x16x32_bf16 v[128:131], a[68:71], a[36:39], v[128:131]\n"
    "ds_read_b128 a[64:67], v201 offset:0\n"
    "v_cvt_pk_bf16_f32 v174, v156, v157\n"
    "v_mfma_f32_16x16x32_bf16 v[128:131], a[72:75], a[40:43], v[128:131]\n"
    "v_exp_f32 v158, v158\n"
    "v_mfma_f32_16x16x32_bf16 v[128:131], a[76:79], a[44:47], v[128:131]\n"
    "ds_read_b128 a[68:71], v201 offset:1024\n"
    "v_mfma_f32_16x16x32_bf16 v[132:135], a[92:95], a[44:47], v[184:187]\n"
    "v_exp_f32 v159, v159\n"
    "v_mfma_f32_16x16x32_bf16 v[132:135], a[88:91], a[40:43], v[132:135]\n"
    "ds_read_b128 a[72:75], v201 offset:2048\n"
    "v_cvt_pk_bf16_f32 v175, v158, v159\n"
    "v_mfma_f32_16x16x32_bf16 v[132:135], a[84:87], a[36:39], v[132:135]\n"
    "v_mov_b32 v246, 0\n"
    "v_mov_b32 v247, 0\n"
    "v_mfma_f32_16x16x32_bf16 v[132:135], a[80:83], a[32:35], v[132:135]\n"
    "ds_read_b128 a[76:79], v201 offset:3072\n"
    "v_and_b32 v252, 0x200, v201\n"
    "v_mfma_f32_16x16x32_bf16 v[136:139], a[96:99], a[32:35], v[184:187]\n"
    "v_lshrrev_b32 v252, 1, v252\n"
    "v_add_u32 v248, v248, v252\n"
    "v_mfma_f32_16x16x32_bf16 v[136:139], a[100:103], a[36:39], v[136:139]\n"
    "ds_read_b128 a[92:95], v201 offset:7168\n"
    "v_mfma_f32_16x16x32_bf16 v[136:139], a[104:107], a[40:43], v[136:139]\n"
    "v_exp_f32 v128, v128\n"
    "v_mfma_f32_16x16x32_bf16 v[136:139], a[108:111], a[44:47], v[136:139]\n"
    "ds_read_b128 a[88:91], v201 offset:6144\n"
    "v_mfma_f32_16x16x32_bf16 v[140:143], a[124:127], a[44:47], v[184:187]\n"
    "v_exp_f32 v129, v129\n"
    "v_mfma_f32_16x16x32_bf16 v[140:143], a[120:123], a[40:43], v[140:143]\n"
    "ds_read_b128 a[84:87], v201 offset:5120\n"
    "v_cvt_pk_bf16_f32 v160, v128, v129\n"
    "v_mfma_f32_16x16x32_bf16 v[140:143], a[116:119], a[36:39], v[140:143]\n"
    "v_exp_f32 v130, v130\n"
    "v_mfma_f32_16x16x32_bf16 v[140:143], a[112:115], a[32:35], v[140:143]\n"
    "ds_read_b128 a[80:83], v201 offset:4096\n"
    "v_mfma_f32_16x16x32_bf16 v[32:35], v[168:171], a[128:131], v[32:35]\n"
    "v_exp_f32 v131, v131\n"
    "v_mfma_f32_16x16x32_bf16 v[32:35], v[172:175], a[132:135], v[32:35]\n"
    "ds_read_b128 a[96:99], v201 offset:8192\n"
    "v_cvt_pk_bf16_f32 v161, v130, v131\n"
    "v_mfma_f32_16x16x32_bf16 v[36:39], v[168:171], a[136:139], v[36:39]\n"
    "v_exp_f32 v132, v132\n"
    "v_mfma_f32_16x16x32_bf16 v[36:39], v[172:175], a[140:143], v[36:39]\n"
    "ds_read_b128 a[100:103], v201 offset:9216\n"
    "v_mfma_f32_16x16x32_bf16 v[40:43], v[168:171], a[144:147], v[40:43]\n"
    "v_exp_f32 v133, v133\n"
    "v_mfma_f32_16x16x32_bf16 v[40:43], v[172:175], a[148:151], v[40:43]\n"
    "ds_read_b128 a[104:107], v201 offset:10240\n"
    "v_cvt_pk_bf16_f32 v162, v132, v133\n"
    "v_mfma_f32_16x16x32_bf16 v[44:47], v[168:171], a[152:155], v[44:47]\n"
    "v_exp_f32 v134, v134\n"
    "v_mfma_f32_16x16x32_bf16 v[44:47], v[172:175], a[156:159], v[44:47]\n"
    "ds_read_b128 a[108:111], v201 offset:11264\n"
    "v_mfma_f32_16x16x32_bf16 v[48:51], v[168:171], a[160:163], v[48:51]\n"
    "v_exp_f32 v135, v135\n"
    "v_mfma_f32_16x16x32_bf16 v[48:51], v[172:175], a[164:167], v[48:51]\n"
    "ds_read_b128 a[124:127], v201 offset:15360\n"
    "v_cvt_pk_bf16_f32 v163, v134, v135\n"
    "v_mfma_f32_16x16x32_bf16 v[52:55], v[168:171], a[168:171], v[52:55]\n"
    "v_exp_f32 v136, v136\n"
    "v_mfma_f32_16x16x32_bf16 v[52:55], v[172:175], a[172:175], v[52:55]\n"
    "ds_read_b128 a[120:123], v201 offset:14336\n"
    "v_mfma_f32_16x16x32_bf16 v[56:59], v[168:171], a[176:179], v[56:59]\n"
    "v_exp_f32 v137, v137\n"
    "v_mfma_f32_16x16x32_bf16 v[56:59], v[172:175], a[180:183], v[56:59]\n"
    "s_waitcnt lgkmcnt(4)\n"
    "ds_read_b128 a[116:119], v201 offset:13312\n"
    "v_cvt_pk_bf16_f32 v164, v136, v137\n"
    "v_mfma_f32_16x16x32_bf16 v[60:63], v[168:171], a[184:187], v[60:63]\n"
    "v_exp_f32 v138, v138\n"
    "v_mfma_f32_16x16x32_bf16 v[60:63], v[172:175], a[188:191], v[60:63]\n"
    "ds_read_b128 a[112:115], v201 offset:12288\n"
    "v_smfmac_f32_16x16x64_bf16 a[200:203], a[192:195], v[168:175], v240\n"
    "v_mfma_f32_16x16x32_bf16 v[144:147], a[64:67], a[0:3], v[176:179]\n"
    "v_exp_f32 v139, v139\n"
    "v_mfma_f32_16x16x32_bf16 v[144:147], a[68:71], a[4:7], v[144:147]\n"
    "v_exp_f32 v140, v140\n"
    "v_mfma_f32_16x16x32_bf16 v[144:147], a[72:75], a[8:11], v[144:147]\n"
    "v_cvt_pk_bf16_f32 v165, v138, v139\n"
    "v_mfma_f32_16x16x32_bf16 v[144:147], a[76:79], a[12:15], v[144:147]\n"
    "v_exp_f32 v141, v141\n"
    "v_mfma_f32_16x16x32_bf16 v[148:151], a[92:95], a[12:15], v[176:179]\n"
    "v_cvt_pk_bf16_f32 v166, v140, v141\n"
    "v_mfma_f32_16x16x32_bf16 v[148:151], a[88:91], a[8:11], v[148:151]\n"
    "v_exp_f32 v142, v142\n"
    "v_mfma_f32_16x16x32_bf16 v[148:151], a[84:87], a[4:7], v[148:151]\n"
    "v_exp_f32 v143, v143\n"
    "v_mfma_f32_16x16x32_bf16 v[148:151], a[80:83], a[0:3], v[148:151]\n"
    "v_cvt_pk_bf16_f32 v167, v142, v143\n"
    "v_mfma_f32_16x16x32_bf16 v[152:155], a[96:99], a[0:3], v[176:179]\n"
    "v_mfma_f32_16x16x32_bf16 v[152:155], a[100:103], a[4:7], v[152:155]\n"
    "s_waitcnt lgkmcnt(0)\n"
    "v_mfma_f32_16x16x32_bf16 v[152:155], a[104:107], a[8:11], v[152:155]\n"
    "v_exp_f32 v144, v144\n"
    "v_mfma_f32_16x16x32_bf16 v[152:155], a[108:111], a[12:15], v[152:155]\n"
    "v_exp_f32 v145, v145\n"
    "v_mfma_f32_16x16x32_bf16 v[156:159], a[124:127], a[12:15], v[176:179]\n"
    "v_cvt_pk_bf16_f32 v168, v144, v145\n"
    "v_mfma_f32_16x16x32_bf16 v[156:159], a[120:123], a[8:11], v[156:159]\n"
    "v_exp_f32 v146, v146\n"
    "v_mfma_f32_16x16x32_bf16 v[156:159], a[116:119], a[4:7], v[156:159]\n"
    "v_exp_f32 v147, v147\n"
    "v_mfma_f32_16x16x32_bf16 v[156:159], a[112:115], a[0:3], v[156:159]\n"
    "v_cvt_pk_bf16_f32 v169, v146, v147\n"
    "v_mfma_f32_16x16x32_bf16 v[64:67], v[160:163], a[128:131], v[64:67]\n"
    "v_exp_f32 v148, v148\n"
    "v_mfma_f32_16x16x32_bf16 v[64:67], v[164:167], a[132:135], v[64:67]\n"
    "ds_read_b128 a[128:131], v201 offset:16384\n"
    "v_mfma_f32_16x16x32_bf16 v[68:71], v[160:163], a[136:139], v[68:71]\n"
    "v_exp_f32 v149, v149\n"
    "v_mfma_f32_16x16x32_bf16 v[68:71], v[164:167], a[140:143], v[68:71]\n"
    "ds_read_b128 a[132:135], v201 offset:24576\n"
    "v_cvt_pk_bf16_f32 v170, v148, v149\n"
    "v_mfma_f32_16x16x32_bf16 v[72:75], v[160:163], a[144:147], v[72:75]\n"
    "v_exp_f32 v150, v150\n"
    "v_mfma_f32_16x16x32_bf16 v[72:75], v[164:167], a[148:151], v[72:75]\n"
    "ds_read_b128 a[136:139], v201 offset:17408\n"
    "v_mfma_f32_16x16x32_bf16 v[76:79], v[160:163], a[152:155], v[76:79]\n"
    "v_exp_f32 v151, v151\n"
    "v_mfma_f32_16x16x32_bf16 v[76:79], v[164:167], a[156:159], v[76:79]\n"
    "ds_read_b128 a[140:143], v201 offset:25600\n"
    "v_cvt_pk_bf16_f32 v171, v150, v151\n"
    "v_mfma_f32_16x16x32_bf16 v[80:83], v[160:163], a[160:163], v[80:83]\n"
    "v_exp_f32 v152, v152\n"
    "v_mfma_f32_16x16x32_bf16 v[80:83], v[164:167], a[164:167], v[80:83]\n"
    "ds_read_b128 a[144:147], v201 offset:18432\n"
    "v_mfma_f32_16x16x32_bf16 v[84:87], v[160:163], a[168:171], v[84:87]\n"
    "v_exp_f32 v153, v153\n"
    "v_mfma_f32_16x16x32_bf16 v[84:87], v[164:167], a[172:175], v[84:87]\n"
    "ds_read_b128 a[148:151], v201 offset:26624\n"
    "v_cvt_pk_bf16_f32 v172, v152, v153\n"
    "v_mfma_f32_16x16x32_bf16 v[88:91], v[160:163], a[176:179], v[88:91]\n"
    "v_exp_f32 v154, v154\n"
    "v_mfma_f32_16x16x32_bf16 v[88:91], v[164:167], a[180:183], v[88:91]\n"
    "ds_read_b128 a[152:155], v201 offset:19456\n"
    "v_mfma_f32_16x16x32_bf16 v[92:95], v[160:163], a[184:187], v[92:95]\n"
    "v_exp_f32 v155, v155\n"
    "v_mfma_f32_16x16x32_bf16 v[92:95], v[164:167], a[188:191], v[92:95]\n"
    "ds_read_b128 a[156:159], v201 offset:27648\n"
    "v_cvt_pk_bf16_f32 v173, v154, v155\n"
    "v_smfmac_f32_16x16x64_bf16 a[204:207], a[192:195], v[160:167], v240\n"
    "v_mfma_f32_16x16x32_bf16 v[128:131], a[64:67], a[16:19], v[180:183]\n"
    "ds_read_b128 a[160:163], v201 offset:20480\n"
    "v_mfma_f32_16x16x32_bf16 v[128:131], a[68:71], a[20:23], v[128:131]\n"
    "v_exp_f32 v156, v156\n"
    "v_mfma_f32_16x16x32_bf16 v[128:131], a[72:75], a[24:27], v[128:131]\n"
    "ds_read_b128 a[64:67], v201 offset:32768\n"
    "v_mfma_f32_16x16x32_bf16 v[128:131], a[76:79], a[28:31], v[128:131]\n"
    "ds_read_b128 a[164:167], v201 offset:28672\n"
    "v_mfma_f32_16x16x32_bf16 v[132:135], a[92:95], a[28:31], v[180:183]\n"
    "ds_read_b128 a[68:71], v201 offset:33792\n"
    "v_mfma_f32_16x16x32_bf16 v[132:135], a[88:91], a[24:27], v[132:135]\n"
    "ds_read_b128 a[168:171], v201 offset:21504\n"
    "v_mfma_f32_16x16x32_bf16 v[132:135], a[84:87], a[20:23], v[132:135]\n"
    "v_exp_f32 v157, v157\n"
    "v_mfma_f32_16x16x32_bf16 v[132:135], a[80:83], a[16:19], v[132:135]\n"
    "ds_read_b128 a[72:75], v201 offset:34816\n"
    "v_cvt_pk_bf16_f32 v174, v156, v157\n"
    "v_mfma_f32_16x16x32_bf16 v[136:139], a[96:99], a[16:19], v[180:183]\n"
    "v_exp_f32 v158, v158\n"
    "v_mfma_f32_16x16x32_bf16 v[136:139], a[100:103], a[20:23], v[136:139]\n"
    "ds_read_b128 a[172:175], v201 offset:29696\n"
    "v_mfma_f32_16x16x32_bf16 v[136:139], a[104:107], a[24:27], v[136:139]\n"
    "v_exp_f32 v159, v159\n"
    "v_mfma_f32_16x16x32_bf16 v[136:139], a[108:111], a[28:31], v[136:139]\n"
    "s_waitcnt lgkmcnt(5)\n"
    "ds_read_b128 a[76:79], v201 offset:35840\n"
    "v_cvt_pk_bf16_f32 v175, v158, v159\n"
    "v_mfma_f32_16x16x32_bf16 v[140:143], a[124:127], a[28:31], v[180:183]\n"
    "ds_read_b128 a[176:179], v201 offset:22528\n"
    "v_mfma_f32_16x16x32_bf16 v[140:143], a[120:123], a[24:27], v[140:143]\n"
    "v_exp_f32 v128, v128\n"
    "v_mfma_f32_16x16x32_bf16 v[140:143], a[116:119], a[20:23], v[140:143]\n"
    "ds_read_b128 a[92:95], v201 offset:39936\n"
    "v_mfma_f32_16x16x32_bf16 v[140:143], a[112:115], a[16:19], v[140:143]\n"
    "v_exp_f32 v129, v129\n"
    "v_mfma_f32_16x16x32_bf16 v[0:3], v[168:171], a[128:131], v[0:3]\n"
    "ds_read_b128 a[180:183], v201 offset:30720\n"
    "v_cvt_pk_bf16_f32 v160, v128, v129\n"
    "v_mfma_f32_16x16x32_bf16 v[0:3], v[172:175], a[132:135], v[0:3]\n"
    "v_exp_f32 v130, v130\n"
    "v_mfma_f32_16x16x32_bf16 v[4:7], v[168:171], a[136:139], v[4:7]\n"
    "ds_read_b128 a[88:91], v201 offset:38912\n"
    "v_mfma_f32_16x16x32_bf16 v[4:7], v[172:175], a[140:143], v[4:7]\n"
    "v_exp_f32 v131, v131\n"
    "v_mfma_f32_16x16x32_bf16 v[8:11], v[168:171], a[144:147], v[8:11]\n"
    "ds_read_b128 a[184:187], v201 offset:23552\n"
    "v_cvt_pk_bf16_f32 v161, v130, v131\n"
    "v_mfma_f32_16x16x32_bf16 v[8:11], v[172:175], a[148:151], v[8:11]\n"
    "v_exp_f32 v132, v132\n"
    "v_mfma_f32_16x16x32_bf16 v[12:15], v[168:171], a[152:155], v[12:15]\n"
    "ds_read_b128 a[84:87], v201 offset:37888\n"
    "v_mfma_f32_16x16x32_bf16 v[12:15], v[172:175], a[156:159], v[12:15]\n"
    "v_exp_f32 v133, v133\n"
    "v_mfma_f32_16x16x32_bf16 v[16:19], v[168:171], a[160:163], v[16:19]\n"
    "s_waitcnt lgkmcnt(4)\n"
    "ds_read_b128 a[188:191], v201 offset:31744\n"
    "v_cvt_pk_bf16_f32 v162, v132, v133\n"
    "v_mfma_f32_16x16x32_bf16 v[16:19], v[172:175], a[164:167], v[16:19]\n"
    "206: s_cbranch_execz 207f\n"
    "v_exp_f32 v134, v134\n"
    "v_mfma_f32_16x16x32_bf16 v[20:23], v[168:171], a[168:171], v[20:23]\n"
    "ds_read_b128 a[80:83], v201 offset:36864\n"
    "v_mfma_f32_16x16x32_bf16 v[20:23], v[172:175], a[172:175], v[20:23]\n"
    "v_exp_f32 v135, v135\n"
    "v_mfma_f32_16x16x32_bf16 v[24:27], v[168:171], a[176:179], v[24:27]\n"
    "s_waitcnt lgkmcnt(4)\n"
    "ds_read_b128 a[96:99], v201 offset:40960\n"
    "v_cvt_pk_bf16_f32 v163, v134, v135\n"
    "v_mfma_f32_16x16x32_bf16 v[24:27], v[172:175], a[180:183], v[24:27]\n"
    "s_waitcnt lgkmcnt(4)\n"
    "v_exp_f32 v136, v136\n"
    "v_mfma_f32_16x16x32_bf16 v[28:31], v[168:171], a[184:187], v[28:31]\n"
    "s_waitcnt lgkmcnt(2)\n"
    "ds_read_b128 a[100:103], v201 offset:41984\n"
    "v_mfma_f32_16x16x32_bf16 v[28:31], v[172:175], a[188:191], v[28:31]\n"
    "v_exp_f32 v137, v137\n"
    "v_smfmac_f32_16x16x64_bf16 a[196:199], a[192:195], v[168:175], v240\n"
    "ds_read_b128 a[104:107], v201 offset:43008\n"
    "v_mfma_f32_16x16x32_bf16 v[144:147], a[64:67], a[0:3], v[176:179]\n"
    "v_exp_f32 v138, v138\n"
    "v_mfma_f32_16x16x32_bf16 v[144:147], a[68:71], a[4:7], v[144:147]\n"
    "ds_read_b128 a[108:111], v201 offset:44032\n"
    "v_cvt_pk_bf16_f32 v164, v136, v137\n"
    "v_mfma_f32_16x16x32_bf16 v[144:147], a[72:75], a[8:11], v[144:147]\n"
    "v_exp_f32 v139, v139\n"
    "v_mfma_f32_16x16x32_bf16 v[144:147], a[76:79], a[12:15], v[144:147]\n"
    "ds_read_b128 a[124:127], v201 offset:48128\n"
    "v_cvt_pk_bf16_f32 v165, v138, v139\n"
    "v_mfma_f32_16x16x32_bf16 v[148:151], a[92:95], a[12:15], v[176:179]\n"
    "v_exp_f32 v140, v140\n"
    "v_mfma_f32_16x16x32_bf16 v[148:151], a[88:91], a[8:11], v[148:151]\n"
    "ds_read_b128 a[120:123], v201 offset:47104\n"
    "v_mfma_f32_16x16x32_bf16 v[148:151], a[84:87], a[4:7], v[148:151]\n"
    "s_waitcnt lgkmcnt(4)\n"
    "v_exp_f32 v141, v141\n"
    "v_mfma_f32_16x16x32_bf16 v[148:151], a[80:83], a[0:3], v[148:151]\n"
    "ds_read_b128 a[116:119], v201 offset:46080\n"
    "v_cvt_pk_bf16_f32 v166, v140, v141\n"
    "v_mfma_f32_16x16x32_bf16 v[152:155], a[96:99], a[0:3], v[176:179]\n"
    "ds_read_b128 a[112:115], v201 offset:45056\n"
    "v_mfma_f32_16x16x32_bf16 v[152:155], a[100:103], a[4:7], v[152:155]\n"
    "s_waitcnt lgkmcnt(5)\n"
    "v_exp_f32 v142, v142\n"
    "v_mfma_f32_16x16x32_bf16 v[152:155], a[104:107], a[8:11], v[152:155]\n"
    "s_waitcnt lgkmcnt(4)\n"
    "v_exp_f32 v143, v143\n"
    "v_mfma_f32_16x16x32_bf16 v[152:155], a[108:111], a[12:15], v[152:155]\n"
    "s_waitcnt lgkmcnt(3)\n"
    "v_exp_f32 v144, v144\n"
    "v_mfma_f32_16x16x32_bf16 v[156:159], a[124:127], a[12:15], v[176:179]\n"
    "s_waitcnt lgkmcnt(2)\n"
    "v_exp_f32 v145, v145\n"
    "v_mfma_f32_16x16x32_bf16 v[156:159], a[120:123], a[8:11], v[156:159]\n"
    "s_waitcnt lgkmcnt(1)\n"
    "v_exp_f32 v146, v146\n"
    "v_mfma_f32_16x16x32_bf16 v[156:159], a[116:119], a[4:7], v[156:159]\n"
    "s_waitcnt lgkmcnt(0)\n"
    "v_exp_f32 v147, v147\n"
    "v_cvt_pk_bf16_f32 v167, v142, v143\n"
    "v_mfma_f32_16x16x32_bf16 v[156:159], a[112:115], a[0:3], v[156:159]\n"
    "v_cvt_pk_bf16_f32 v168, v144, v145\n"
    "v_cvt_pk_bf16_f32 v169, v146, v147\n"
    "v_mfma_f32_16x16x32_bf16 v[32:35], v[160:163], a[128:131], v[32:35]\n"
    "v_exp_f32 v148, v148\n"
    "v_mfma_f32_16x16x32_bf16 v[32:35], v[164:167], a[132:135], v[32:35]\n"
    "ds_read_b128 a[128:131], v201 offset:49152\n"
    "v_mfma_f32_16x16x32_bf16 v[36:39], v[160:163], a[136:139], v[36:39]\n"
    "v_exp_f32 v149, v149\n"
    "v_mfma_f32_16x16x32_bf16 v[36:39], v[164:167], a[140:143], v[36:39]\n"
    "ds_read_b128 a[132:135], v201 offset:57344\n"
    "v_cvt_pk_bf16_f32 v170, v148, v149\n"
    "v_mfma_f32_16x16x32_bf16 v[40:43], v[160:163], a[144:147], v[40:43]\n"
    "v_exp_f32 v150, v150\n"
    "v_mfma_f32_16x16x32_bf16 v[40:43], v[164:167], a[148:151], v[40:43]\n"
    "ds_read_b128 a[136:139], v201 offset:50176\n"
    "v_mfma_f32_16x16x32_bf16 v[44:47], v[160:163], a[152:155], v[44:47]\n"
    "v_exp_f32 v151, v151\n"
    "v_mfma_f32_16x16x32_bf16 v[44:47], v[164:167], a[156:159], v[44:47]\n"
    "ds_read_b128 a[140:143], v201 offset:58368\n"
    "v_cvt_pk_bf16_f32 v171, v150, v151\n"
    "v_mfma_f32_16x16x32_bf16 v[48:51], v[160:163], a[160:163], v[48:51]\n"
    "v_exp_f32 v152, v152\n"
    "v_mfma_f32_16x16x32_bf16 v[48:51], v[164:167], a[164:167], v[48:51]\n"
    "ds_read_b128 a[144:147], v201 offset:51200\n"
    "v_mfma_f32_16x16x32_bf16 v[52:55], v[160:163], a[168:171], v[52:55]\n"
    "v_exp_f32 v153, v153\n"
    "v_mfma_f32_16x16x32_bf16 v[52:55], v[164:167], a[172:175], v[52:55]\n"
    "ds_read_b128 a[148:151], v201 offset:59392\n"
    "v_cvt_pk_bf16_f32 v172, v152, v153\n"
    "v_mfma_f32_16x16x32_bf16 v[56:59], v[160:163], a[176:179], v[56:59]\n"
    "v_exp_f32 v154, v154\n"
    "v_mfma_f32_16x16x32_bf16 v[56:59], v[164:167], a[180:183], v[56:59]\n"
    "ds_read_b128 a[152:155], v201 offset:52224\n"
    "v_mfma_f32_16x16x32_bf16 v[60:63], v[160:163], a[184:187], v[60:63]\n"
    "v_exp_f32 v155, v155\n"
    "v_mfma_f32_16x16x32_bf16 v[60:63], v[164:167], a[188:191], v[60:63]\n"
    "ds_read_b128 a[156:159], v201 offset:60416\n"
    "v_cvt_pk_bf16_f32 v173, v154, v155\n"
    "v_smfmac_f32_16x16x64_bf16 a[200:203], a[192:195], v[160:167], v240\n"
    "v_mfma_f32_16x16x32_bf16 v[128:131], a[64:67], a[48:51], v[188:191]\n"
    "ds_read_b128 a[160:163], v201 offset:53248\n"
    "v_mfma_f32_16x16x32_bf16 v[128:131], a[68:71], a[52:55], v[128:131]\n"
    "v_exp_f32 v156, v156\n"
    "v_mfma_f32_16x16x32_bf16 v[128:131], a[72:75], a[56:59], v[128:131]\n"
    "ds_read_b128 a[164:167], v201 offset:61440\n"
    "v_mfma_f32_16x16x32_bf16 v[128:131], a[76:79], a[60:63], v[128:131]\n"
    "v_exp_f32 v157, v157\n"
    "v_mfma_f32_16x16x32_bf16 v[132:135], a[92:95], a[60:63], v[188:191]\n"
    "ds_read_b128 a[168:171], v201 offset:54272\n"
    "v_cvt_pk_bf16_f32 v174, v156, v157\n"
    "v_mfma_f32_16x16x32_bf16 v[132:135], a[88:91], a[56:59], v[132:135]\n"
    "v_exp_f32 v158, v158\n"
    "v_mfma_f32_16x16x32_bf16 v[132:135], a[84:87], a[52:55], v[132:135]\n"
    "ds_read_b128 a[172:175], v201 offset:62464\n"
    "v_mfma_f32_16x16x32_bf16 v[132:135], a[80:83], a[48:51], v[132:135]\n"
    "v_exp_f32 v159, v159\n"
    "v_mfma_f32_16x16x32_bf16 v[136:139], a[96:99], a[48:51], v[188:191]\n"
    "ds_read_b128 a[176:179], v201 offset:55296\n"
    "v_cvt_pk_bf16_f32 v175, v158, v159\n"
    "v_mfma_f32_16x16x32_bf16 v[136:139], a[100:103], a[52:55], v[136:139]\n"
    "v_mfma_f32_16x16x32_bf16 v[136:139], a[104:107], a[56:59], v[136:139]\n"
    "v_exp_f32 v128, v128\n"
    "v_mfma_f32_16x16x32_bf16 v[136:139], a[108:111], a[60:63], v[136:139]\n"
    "ds_read_b128 a[180:183], v201 offset:63488\n"
    "v_mfma_f32_16x16x32_bf16 v[140:143], a[124:127], a[60:63], v[188:191]\n"
    "v_exp_f32 v129, v129\n"
    "v_mfma_f32_16x16x32_bf16 v[140:143], a[120:123], a[56:59], v[140:143]\n"
    "ds_read_b128 a[184:187], v201 offset:56320\n"
    "v_cvt_pk_bf16_f32 v160, v128, v129\n"
    "v_mfma_f32_16x16x32_bf16 v[140:143], a[116:119], a[52:55], v[140:143]\n"
    "v_exp_f32 v130, v130\n"
    "v_mfma_f32_16x16x32_bf16 v[140:143], a[112:115], a[48:51], v[140:143]\n"
    "s_waitcnt lgkmcnt(3)\n"
    "ds_read_b128 a[188:191], v201 offset:64512\n"
    "v_smfmac_f32_16x16x64_bf16 a[196:199], a[192:195], v[168:175], v240\n"
    "v_mfma_f32_16x16x32_bf16 v[0:3], v[168:171], a[128:131], v[0:3]\n"
    "v_exp_f32 v131, v131\n"
    "v_mfma_f32_16x16x32_bf16 v[0:3], v[172:175], a[132:135], v[0:3]\n"
    "v_exp_f32 v132, v132\n"
    "v_mfma_f32_16x16x32_bf16 v[4:7], v[168:171], a[136:139], v[4:7]\n"
    "v_cvt_pk_bf16_f32 v161, v130, v131\n"
    "v_mfma_f32_16x16x32_bf16 v[4:7], v[172:175], a[140:143], v[4:7]\n"
    "v_exp_f32 v133, v133\n"
    "v_mfma_f32_16x16x32_bf16 v[8:11], v[168:171], a[144:147], v[8:11]\n"
    "v_cvt_pk_bf16_f32 v162, v132, v133\n"
    "s_nop 1\n"
    "v_accvgpr_read_b32 v192, a196\n"
    "v_accvgpr_read_b32 v193, a197\n"
    "v_add_f32 v192, v192, v193\n"
    "v_mfma_f32_16x16x32_bf16 v[8:11], v[172:175], a[148:151], v[8:11]\n"
    "v_exp_f32 v134, v134\n"
    "v_mfma_f32_16x16x4_f32 v[220:223], v192, v228, 0\n"
    "v_mfma_f32_16x16x32_bf16 v[12:15], v[168:171], a[152:155], v[12:15]\n"
    "v_exp_f32 v135, v135\n"
    "v_mfma_f32_16x16x32_bf16 v[12:15], v[172:175], a[156:159], v[12:15]\n"
    "v_cvt_pk_bf16_f32 v163, v134, v135\n"
    "v_mfma_f32_16x16x32_bf16 v[16:19], v[168:171], a[160:163], v[16:19]\n"
    "v_exp_f32 v136, v136\n"
    "v_mfma_f32_16x16x32_bf16 v[16:19], v[172:175], a[164:167], v[16:19]\n"
    "s_nop 2\n"
    "v_rcp_f32 v220, v220\n"
    "v_mfma_f32_16x16x32_bf16 v[20:23], v[168:171], a[168:171], v[20:23]\n"
    "v_rcp_f32 v221, v221\n"
    "v_mfma_f32_16x16x32_bf16 v[20:23], v[172:175], a[172:175], v[20:23]\n"
    "s_waitcnt lgkmcnt(0)\n"
    "v_exp_f32 v137, v137\n"
    "v_mfma_f32_16x16x32_bf16 v[24:27], v[168:171], a[176:179], v[24:27]\n"
    "v_cvt_pk_bf16_f32 v164, v136, v137\n"
    "v_mfma_f32_16x16x32_bf16 v[24:27], v[172:175], a[180:183], v[24:27]\n"
    "v_exp_f32 v138, v138\n"
    "v_mfma_f32_16x16x32_bf16 v[28:31], v[168:171], a[184:187], v[28:31]\n"
    "v_exp_f32 v139, v139\n"
    "v_mfma_f32_16x16x32_bf16 v[28:31], v[172:175], a[188:191], v[28:31]\n"
    "v_cvt_pk_bf16_f32 v165, v138, v139\n"
    "v_mfma_f32_16x16x32_bf16 v[144:147], a[64:67], a[16:19], v[180:183]\n"
    "v_exp_f32 v140, v140\n"
    "v_mfma_f32_16x16x32_bf16 v[144:147], a[68:71], a[20:23], v[144:147]\n"
    "v_exp_f32 v141, v141\n"
    "v_mfma_f32_16x16x32_bf16 v[144:147], a[72:75], a[24:27], v[144:147]\n"
    "v_cvt_pk_bf16_f32 v166, v140, v141\n"
    "v_rcp_f32 v222, v222\n"
    "v_rcp_f32 v223, v223\n"
    "v_pk_mul_f32 v[0:1], v[0:1], v[220:221]\n"
    "v_pk_mul_f32 v[4:5], v[4:5], v[220:221]\n"
    "v_pk_mul_f32 v[8:9], v[8:9], v[220:221]\n"
    "v_pk_mul_f32 v[12:13], v[12:13], v[220:221]\n"
    "v_pk_mul_f32 v[16:17], v[16:17], v[220:221]\n"
    "v_pk_mul_f32 v[20:21], v[20:21], v[220:221]\n"
    "v_pk_mul_f32 v[24:25], v[24:25], v[220:221]\n"
    "v_pk_mul_f32 v[28:29], v[28:29], v[220:221]\n"
    "v_pk_mul_f32 v[2:3], v[2:3], v[222:223]\n"
    "v_pk_mul_f32 v[6:7], v[6:7], v[222:223]\n"
    "v_pk_mul_f32 v[10:11], v[10:11], v[222:223]\n"
    "v_pk_mul_f32 v[14:15], v[14:15], v[222:223]\n"
    "v_pk_mul_f32 v[18:19], v[18:19], v[222:223]\n"
    "v_pk_mul_f32 v[22:23], v[22:23], v[222:223]\n"
    "v_pk_mul_f32 v[26:27], v[26:27], v[222:223]\n"
    "v_pk_mul_f32 v[30:31], v[30:31], v[222:223]\n"
    "v_mfma_f32_16x16x32_bf16 v[144:147], a[76:79], a[28:31], v[144:147]\n"
    "v_cvt_pk_bf16_f32 v204, v0, v4\n"
    "v_cvt_pk_bf16_f32 v205, v8, v12\n"
    "v_mfma_f32_16x16x32_bf16 v[148:151], a[92:95], a[28:31], v[180:183]\n"
    "v_cvt_pk_bf16_f32 v206, v16, v20\n"
    "v_cvt_pk_bf16_f32 v207, v24, v28\n"
    "v_mfma_f32_16x16x32_bf16 v[148:151], a[88:91], a[24:27], v[148:151]\n"
    "global_store_dwordx4 v250, v[204:207], s[46:47] offset:0 sc1 nt\n"
    "v_cvt_pk_bf16_f32 v208, v1, v5\n"
    "v_mfma_f32_16x16x32_bf16 v[148:151], a[84:87], a[20:23], v[148:151]\n"
    "v_exp_f32 v142, v142\n"
    "v_mfma_f32_16x16x32_bf16 v[148:151], a[80:83], a[16:19], v[148:151]\n"
    "v_exp_f32 v143, v143\n"
    "v_mfma_f32_16x16x32_bf16 v[152:155], a[96:99], a[16:19], v[180:183]\n"
    "v_cvt_pk_bf16_f32 v167, v142, v143\n"
    "v_cvt_pk_bf16_f32 v209, v9, v13\n"
    "v_mfma_f32_16x16x32_bf16 v[152:155], a[100:103], a[20:23], v[152:155]\n"
    "v_cvt_pk_bf16_f32 v210, v17, v21\n"
    "v_cvt_pk_bf16_f32 v211, v25, v29\n"
    "v_mfma_f32_16x16x32_bf16 v[152:155], a[104:107], a[24:27], v[152:155]\n"
    "global_store_dwordx4 v250, v[208:211], s[46:47] offset:256 sc1 nt\n"
    "v_cvt_pk_bf16_f32 v212, v2, v6\n"
    "v_mfma_f32_16x16x32_bf16 v[152:155], a[108:111], a[28:31], v[152:155]\n"
    "v_exp_f32 v144, v144\n"
    "v_mfma_f32_16x16x32_bf16 v[156:159], a[124:127], a[28:31], v[180:183]\n"
    "v_exp_f32 v145, v145\n"
    "v_mfma_f32_16x16x32_bf16 v[156:159], a[120:123], a[24:27], v[156:159]\n"
    "v_cvt_pk_bf16_f32 v168, v144, v145\n"
    "v_cvt_pk_bf16_f32 v213, v10, v14\n"
    "v_mfma_f32_16x16x32_bf16 v[156:159], a[116:119], a[20:23], v[156:159]\n"
    "v_exp_f32 v146, v146\n"
    "v_mfma_f32_16x16x32_bf16 v[156:159], a[112:115], a[16:19], v[156:159]\n"
    "v_cvt_pk_bf16_f32 v214, v18, v22\n"
    "v_cvt_pk_bf16_f32 v215, v26, v30\n"
    "v_mfma_f32_16x16x32_bf16 v[96:99], v[160:163], a[128:131], v[96:99]\n"
    "global_store_dwordx4 v250, v[212:215], s[46:47] offset:512 sc1 nt\n"
    "v_cvt_pk_bf16_f32 v216, v3, v7\n"
    "v_mfma_f32_16x16x32_bf16 v[96:99], v[164:167], a[132:135], v[96:99]\n"
    "v_exp_f32 v147, v147\n"
    "v_mfma_f32_16x16x32_bf16 v[100:103], v[160:163], a[136:139], v[100:103]\n"
    "v_exp_f32 v148, v148\n"
    "v_mfma_f32_16x16x32_bf16 v[100:103], v[164:167], a[140:143], v[100:103]\n"
    "v_cvt_pk_bf16_f32 v169, v146, v147\n"
    "v_cvt_pk_bf16_f32 v217, v11, v15\n"
    "v_mfma_f32_16x16x32_bf16 v[104:107], v[160:163], a[144:147], v[104:107]\n"
    "v_exp_f32 v149, v149\n"
    "v_mfma_f32_16x16x32_bf16 v[104:107], v[164:167], a[148:151], v[104:107]\n"
    "v_cvt_pk_bf16_f32 v170, v148, v149\n"
    "v_cvt_pk_bf16_f32 v218, v19, v23\n"
    "v_mfma_f32_16x16x32_bf16 v[108:111], v[160:163], a[152:155], v[108:111]\n"
    "v_exp_f32 v150, v150\n"
    "v_mfma_f32_16x16x32_bf16 v[108:111], v[164:167], a[156:159], v[108:111]\n"
    "v_cvt_pk_bf16_f32 v219, v27, v31\n"
    "v_mfma_f32_16x16x32_bf16 v[112:115], v[160:163], a[160:163], v[112:115]\n"
    "global_store_dwordx4 v250, v[216:219], s[46:47] offset:768 sc1 nt\n"
    "v_mfma_f32_16x16x32_bf16 v[112:115], v[164:167], a[164:167], v[112:115]\n"
    "v_exp_f32 v151, v151\n"
    "v_mfma_f32_16x16x32_bf16 v[116:119], v[160:163], a[168:171], v[116:119]\n"
    "v_exp_f32 v152, v152\n"
    "v_mfma_f32_16x16x32_bf16 v[116:119], v[164:167], a[172:175], v[116:119]\n"
    "v_cvt_pk_bf16_f32 v171, v150, v151\n"
    "v_mfma_f32_16x16x32_bf16 v[120:123], v[160:163], a[176:179], v[120:123]\n"
    "v_exp_f32 v153, v153\n"
    "v_mfma_f32_16x16x32_bf16 v[120:123], v[164:167], a[180:183], v[120:123]\n"
    "v_cvt_pk_bf16_f32 v172, v152, v153\n"
    "v_mfma_f32_16x16x32_bf16 v[124:127], v[160:163], a[184:187], v[124:127]\n"
    "v_exp_f32 v154, v154\n"
    "v_mfma_f32_16x16x32_bf16 v[124:127], v[164:167], a[188:191], v[124:127]\n"
    "v_exp_f32 v155, v155\n"
    "v_smfmac_f32_16x16x64_bf16 a[208:211], a[192:195], v[160:167], v240\n"
    "v_cvt_pk_bf16_f32 v173, v154, v155\n"
    "v_mfma_f32_16x16x32_bf16 v[128:131], a[64:67], a[32:35], v[184:187]\n"
    "v_exp_f32 v156, v156\n"
    "v_mfma_f32_16x16x32_bf16 v[128:131], a[68:71], a[36:39], v[128:131]\n"
    "ds_read_b128 a[64:67], v201 offset:0\n"
    "v_mfma_f32_16x16x32_bf16 v[128:131], a[72:75], a[40:43], v[128:131]\n"
    "v_exp_f32 v157, v157\n"
    "v_mfma_f32_16x16x32_bf16 v[128:131], a[76:79], a[44:47], v[128:131]\n"
    "ds_read_b128 a[68:71], v201 offset:1024\n"
    "v_cvt_pk_bf16_f32 v174, v156, v157\n"
    "v_mfma_f32_16x16x32_bf16 v[132:135], a[92:95], a[44:47], v[184:187]\n"
    "v_exp_f32 v158, v158\n"
    "v_mfma_f32_16x16x32_bf16 v[132:135], a[88:91], a[40:43], v[132:135]\n"
    "ds_read_b128 a[72:75], v201 offset:2048\n"
    "v_mfma_f32_16x16x32_bf16 v[132:135], a[84:87], a[36:39], v[132:135]\n"
    "v_exp_f32 v159, v159\n"
    "v_mfma_f32_16x16x32_bf16 v[132:135], a[80:83], a[32:35], v[132:135]\n"
    "ds_read_b128 a[76:79], v201 offset:3072\n"
    "v_cvt_pk_bf16_f32 v175, v158, v159\n"
    "v_mfma_f32_16x16x32_bf16 v[136:139], a[96:99], a[32:35], v[184:187]\n"
    "v_mfma_f32_16x16x32_bf16 v[136:139], a[100:103], a[36:39], v[136:139]\n"
    "ds_read_b128 a[92:95], v201 offset:7168\n"
    "v_mfma_f32_16x16x32_bf16 v[136:139], a[104:107], a[40:43], v[136:139]\n"
    "v_exp_f32 v128, v128\n"
    "v_mfma_f32_16x16x32_bf16 v[136:139], a[108:111], a[44:47], v[136:139]\n"
    "ds_read_b128 a[88:91], v201 offset:6144\n"
    "v_mfma_f32_16x16x32_bf16 v[140:143], a[124:127], a[44:47], v[184:187]\n"
    "v_exp_f32 v129, v129\n"
    "v_mfma_f32_16x16x32_bf16 v[140:143], a[120:123], a[40:43], v[140:143]\n"
    "ds_read_b128 a[84:87], v201 offset:5120\n"
    "v_cvt_pk_bf16_f32 v160, v128, v129\n"
    "v_mfma_f32_16x16x32_bf16 v[140:143], a[116:119], a[36:39], v[140:143]\n"
    "v_exp_f32 v130, v130\n"
    "v_mfma_f32_16x16x32_bf16 v[140:143], a[112:115], a[32:35], v[140:143]\n"
    "ds_read_b128 a[80:83], v201 offset:4096\n"
    "v_smfmac_f32_16x16x64_bf16 a[200:203], a[192:195], v[168:175], v240\n"
    "v_mfma_f32_16x16x32_bf16 v[32:35], v[168:171], a[128:131], v[32:35]\n"
    "v_exp_f32 v131, v131\n"
    "v_mfma_f32_16x16x32_bf16 v[32:35], v[172:175], a[132:135], v[32:35]\n"
    "ds_read_b128 a[96:99], v201 offset:8192\n"
    "v_cvt_pk_bf16_f32 v161, v130, v131\n"
    "v_mfma_f32_16x16x32_bf16 v[36:39], v[168:171], a[136:139], v[36:39]\n"
    "v_exp_f32 v132, v132\n"
    "v_mfma_f32_16x16x32_bf16 v[36:39], v[172:175], a[140:143], v[36:39]\n"
    "ds_read_b128 a[100:103], v201 offset:9216\n"
    "v_mfma_f32_16x16x32_bf16 v[40:43], v[168:171], a[144:147], v[40:43]\n"
    "v_exp_f32 v133, v133\n"
    "s_nop 0\n"
    "v_accvgpr_read_b32 v194, a200\n"
    "v_accvgpr_read_b32 v195, a201\n"
    "v_add_f32 v194, v194, v195\n"
    "v_mfma_f32_16x16x32_bf16 v[40:43], v[172:175], a[148:151], v[40:43]\n"
    "ds_read_b128 a[104:107], v201 offset:10240\n"
    "v_cvt_pk_bf16_f32 v162, v132, v133\n"
    "v_mfma_f32_16x16x4_f32 v[224:227], v194, v228, 0\n"
    "v_mfma_f32_16x16x32_bf16 v[44:47], v[168:171], a[152:155], v[44:47]\n"
    "v_exp_f32 v134, v134\n"
    "v_mfma_f32_16x16x32_bf16 v[44:47], v[172:175], a[156:159], v[44:47]\n"
    "ds_read_b128 a[108:111], v201 offset:11264\n"
    "v_mfma_f32_16x16x32_bf16 v[48:51], v[168:171], a[160:163], v[48:51]\n"
    "v_exp_f32 v135, v135\n"
    "v_mfma_f32_16x16x32_bf16 v[48:51], v[172:175], a[164:167], v[48:51]\n"
    "ds_read_b128 a[124:127], v201 offset:15360\n"
    "v_cvt_pk_bf16_f32 v163, v134, v135\n"
    "v_mfma_f32_16x16x32_bf16 v[52:55], v[168:171], a[168:171], v[52:55]\n"
    "v_exp_f32 v136, v136\n"
    "v_mfma_f32_16x16x32_bf16 v[52:55], v[172:175], a[172:175], v[52:55]\n"
    "ds_read_b128 a[120:123], v201 offset:14336\n"
    "v_mfma_f32_16x16x32_bf16 v[56:59], v[168:171], a[176:179], v[56:59]\n"
    "v_exp_f32 v137, v137\n"
    "v_mfma_f32_16x16x32_bf16 v[56:59], v[172:175], a[180:183], v[56:59]\n"
    "ds_read_b128 a[116:119], v201 offset:13312\n"
    "v_cvt_pk_bf16_f32 v164, v136, v137\n"
    "v_mfma_f32_16x16x32_bf16 v[60:63], v[168:171], a[184:187], v[60:63]\n"
    "v_exp_f32 v138, v138\n"
    "v_rcp_f32 v224, v224\n"
    "v_mfma_f32_16x16x32_bf16 v[60:63], v[172:175], a[188:191], v[60:63]\n"
    "s_waitcnt lgkmcnt(4)\n"
    "ds_read_b128 a[112:115], v201 offset:12288\n"
    "v_xor_b32 v250, 0x1000, v250\n"
    "v_rcp_f32 v225, v225\n"
    "v_mfma_f32_16x16x32_bf16 v[144:147], a[64:67], a[32:35], v[184:187]\n"
    "v_exp_f32 v139, v139\n"
    "v_mfma_f32_16x16x32_bf16 v[144:147], a[68:71], a[36:39], v[144:147]\n"
    "v_exp_f32 v140, v140\n"
    "v_mfma_f32_16x16x32_bf16 v[144:147], a[72:75], a[40:43], v[144:147]\n"
    "v_cvt_pk_bf16_f32 v165, v138, v139\n"
    "v_rcp_f32 v226, v226\n"
    "v_rcp_f32 v227, v227\n"
    "v_pk_mul_f32 v[32:33], v[32:33], v[224:225]\n"
    "v_pk_mul_f32 v[36:37], v[36:37], v[224:225]\n"
    "v_pk_mul_f32 v[40:41], v[40:41], v[224:225]\n"
    "v_pk_mul_f32 v[44:45], v[44:45], v[224:225]\n"
    "v_pk_mul_f32 v[48:49], v[48:49], v[224:225]\n"
    "v_pk_mul_f32 v[52:53], v[52:53], v[224:225]\n"
    "v_pk_mul_f32 v[56:57], v[56:57], v[224:225]\n"
    "v_pk_mul_f32 v[60:61], v[60:61], v[224:225]\n"
    "v_pk_mul_f32 v[34:35], v[34:35], v[226:227]\n"
    "v_pk_mul_f32 v[38:39], v[38:39], v[226:227]\n"
    "v_pk_mul_f32 v[42:43], v[42:43], v[226:227]\n"
    "v_pk_mul_f32 v[46:47], v[46:47], v[226:227]\n"
    "v_pk_mul_f32 v[50:51], v[50:51], v[226:227]\n"
    "v_pk_mul_f32 v[54:55], v[54:55], v[226:227]\n"
    "v_pk_mul_f32 v[58:59], v[58:59], v[226:227]\n"
    "v_pk_mul_f32 v[62:63], v[62:63], v[226:227]\n"
    "v_mfma_f32_16x16x32_bf16 v[144:147], a[76:79], a[44:47], v[144:147]\n"
    "v_exp_f32 v141, v141\n"
    "v_cvt_pk_bf16_f32 v204, v32, v36\n"
    "v_mfma_f32_16x16x32_bf16 v[148:151], a[92:95], a[44:47], v[184:187]\n"
    "v_cvt_pk_bf16_f32 v166, v140, v141\n"
    "v_cvt_pk_bf16_f32 v205, v40, v44\n"
    "v_mfma_f32_16x16x32_bf16 v[148:151], a[88:91], a[40:43], v[148:151]\n"
    "v_exp_f32 v142, v142\n"
    "v_mfma_f32_16x16x32_bf16 v[148:151], a[84:87], a[36:39], v[148:151]\n"
    "v_cvt_pk_bf16_f32 v206, v48, v52\n"
    "v_cvt_pk_bf16_f32 v207, v56, v60\n"
    "v_mfma_f32_16x16x32_bf16 v[148:151], a[80:83], a[32:35], v[148:151]\n"
    "global_store_dwordx4 v250, v[204:207], s[46:47] offset:0 sc1 nt\n"
    "v_cvt_pk_bf16_f32 v208, v33, v37\n"
    "v_mfma_f32_16x16x32_bf16 v[152:155], a[96:99], a[32:35], v[184:187]\n"
    "v_exp_f32 v143, v143\n"
    "v_mfma_f32_16x16x32_bf16 v[152:155], a[100:103], a[36:39], v[152:155]\n"
    "v_cvt_pk_bf16_f32 v167, v142, v143\n"
    "v_cvt_pk_bf16_f32 v209, v41, v45\n"
    "v_mfma_f32_16x16x32_bf16 v[152:155], a[104:107], a[40:43], v[152:155]\n"
    "s_waitcnt lgkmcnt(0)\n"
    "v_exp_f32 v144, v144\n"
    "v_mfma_f32_16x16x32_bf16 v[152:155], a[108:111], a[44:47], v[152:155]\n"
    "v_cvt_pk_bf16_f32 v210, v49, v53\n"
    "v_cvt_pk_bf16_f32 v211, v57, v61\n"
    "v_mfma_f32_16x16x32_bf16 v[156:159], a[124:127], a[44:47], v[184:187]\n"
    "global_store_dwordx4 v250, v[208:211], s[46:47] offset:256 sc1 nt\n"
    "v_cvt_pk_bf16_f32 v212, v34, v38\n"
    "v_mfma_f32_16x16x32_bf16 v[156:159], a[120:123], a[40:43], v[156:159]\n"
    "v_exp_f32 v145, v145\n"
    "v_mfma_f32_16x16x32_bf16 v[156:159], a[116:119], a[36:39], v[156:159]\n"
    "v_exp_f32 v146, v146\n"
    "v_mfma_f32_16x16x32_bf16 v[156:159], a[112:115], a[32:35], v[156:159]\n"
    "v_cvt_pk_bf16_f32 v168, v144, v145\n"
    "v_cvt_pk_bf16_f32 v213, v42, v46\n"
    "v_mfma_f32_16x16x32_bf16 v[64:67], v[160:163], a[128:131], v[64:67]\n"
    "v_exp_f32 v147, v147\n"
    "v_mfma_f32_16x16x32_bf16 v[64:67], v[164:167], a[132:135], v[64:67]\n"
    "ds_read_b128 a[128:131], v201 offset:16384\n"
    "v_cvt_pk_bf16_f32 v169, v146, v147\n"
    "v_mfma_f32_16x16x32_bf16 v[68:71], v[160:163], a[136:139], v[68:71]\n"
    "v_exp_f32 v148, v148\n"
    "v_cvt_pk_bf16_f32 v214, v50, v54\n"
    "v_mfma_f32_16x16x32_bf16 v[68:71], v[164:167], a[140:143], v[68:71]\n"
    "ds_read_b128 a[132:135], v201 offset:24576\n"
    "v_cvt_pk_bf16_f32 v215, v58, v62\n"
    "v_mfma_f32_16x16x32_bf16 v[72:75], v[160:163], a[144:147], v[72:75]\n"
    "global_store_dwordx4 v250, v[212:215], s[46:47] offset:512 sc1 nt\n"
    "v_cvt_pk_bf16_f32 v216, v35, v39\n"
    "v_mfma_f32_16x16x32_bf16 v[72:75], v[164:167], a[148:151], v[72:75]\n"
    "v_exp_f32 v149, v149\n"
    "v_mfma_f32_16x16x32_bf16 v[76:79], v[160:163], a[152:155], v[76:79]\n"
    "ds_read_b128 a[136:139], v201 offset:17408\n"
    "v_cvt_pk_bf16_f32 v170, v148, v149\n"
    "v_cvt_pk_bf16_f32 v217, v43, v47\n"
    "v_mfma_f32_16x16x32_bf16 v[76:79], v[164:167], a[156:159], v[76:79]\n"
    "v_exp_f32 v150, v150\n"
    "v_mfma_f32_16x16x32_bf16 v[80:83], v[160:163], a[160:163], v[80:83]\n"
    "ds_read_b128 a[140:143], v201 offset:25600\n"
    "v_cvt_pk_bf16_f32 v218, v51, v55\n"
    "v_mfma_f32_16x16x32_bf16 v[80:83], v[164:167], a[164:167], v[80:83]\n"
    "v_exp_f32 v151, v151\n"
    "v_cvt_pk_bf16_f32 v219, v59, v63\n"
    "v_mfma_f32_16x16x32_bf16 v[84:87], v[160:163], a[168:171], v[84:87]\n"
    "global_store_dwordx4 v250, v[216:219], s[46:47] offset:768 sc1 nt\n"
    "v_cvt_pk_bf16_f32 v171, v150, v151\n"
    "v_mfma_f32_16x16x32_bf16 v[84:87], v[164:167], a[172:175], v[84:87]\n"
    "s_cmp_lg_u32 %[w], 0\n"
    "s_cbranch_scc1 32f\n"
    "global_store_dwordx2 v248, v[246:247], s[66:67] offset:0 sc1\n"
    "32:\n"
    "ds_read_b128 a[144:147], v201 offset:18432\n"
    "v_mfma_f32_16x16x32_bf16 v[88:91], v[160:163], a[176:179], v[88:91]\n"
    "ds_read_b128 a[148:151], v201 offset:26624\n"
    "v_mfma_f32_16x16x32_bf16 v[88:91], v[164:167], a[180:183], v[88:91]\n"
    "v_exp_f32 v152, v152\n"
    "v_mfma_f32_16x16x32_bf16 v[92:95], v[160:163], a[184:187], v[92:95]\n"
    "ds_read_b128 a[152:155], v201 offset:19456\n"
    "v_mfma_f32_16x16x32_bf16 v[92:95], v[164:167], a[188:191], v[92:95]\n"
    "v_exp_f32 v153, v153\n"
    "v_smfmac_f32_16x16x64_bf16 a[204:207], a[192:195], v[160:167], v240\n"
    "ds_read_b128 a[156:159], v201 offset:27648\n"
    "v_mfma_f32_16x16x32_bf16 v[128:131], a[64:67], a[48:51], v[188:191]\n"
    "v_exp_f32 v154, v154\n"
    "v_mfma_f32_16x16x32_bf16 v[128:131], a[68:71], a[52:55], v[128:131]\n"
    "ds_read_b128 a[160:163], v201 offset:20480\n"
    "v_cvt_pk_bf16_f32 v172, v152, v153\n"
    "v_mfma_f32_16x16x32_bf16 v[128:131], a[72:75], a[56:59], v[128:131]\n"
    "v_exp_f32 v155, v155\n"
    "v_mfma_f32_16x16x32_bf16 v[128:131], a[76:79], a[60:63], v[128:131]\n"
    "ds_read_b128 a[64:67], v203 offset:0\n"
    "v_cvt_pk_bf16_f32 v173, v154, v155\n"
    "v_mfma_f32_16x16x32_bf16 v[132:135], a[92:95], a[60:63], v[188:191]\n"
    "v_exp_f32 v156, v156\n"
    "v_mfma_f32_16x16x32_bf16 v[132:135], a[88:91], a[56:59], v[132:135]\n"
    "ds_read_b128 a[164:167], v201 offset:28672\n"
    "v_mfma_f32_16x16x32_bf16 v[132:135], a[84:87], a[52:55], v[132:135]\n"
    "v_exp_f32 v157, v157\n"
    "v_mfma_f32_16x16x32_bf16 v[132:135], a[80:83], a[48:51], v[132:135]\n"
    "ds_read_b128 a[68:71], v203 offset:1024\n"
    "v_cvt_pk_bf16_f32 v174, v156, v157\n"
    "v_mfma_f32_16x16x32_bf16 v[136:139], a[96:99], a[48:51], v[188:191]\n"
    "v_exp_f32 v158, v158\n"
    "v_mfma_f32_16x16x32_bf16 v[136:139], a[100:103], a[52:55], v[136:139]\n"
    "ds_read_b128 a[168:171], v201 offset:21504\n"
    "v_mfma_f32_16x16x32_bf16 v[136:139], a[104:107], a[56:59], v[136:139]\n"
    "v_exp_f32 v159, v159\n"
    "v_mfma_f32_16x16x32_bf16 v[136:139], a[108:111], a[60:63], v[136:139]\n"
    "ds_read_b128 a[72:75], v203 offset:2048\n"
    "v_cvt_pk_bf16_f32 v175, v158, v159\n"
    "v_mfma_f32_16x16x32_bf16 v[140:143], a[124:127], a[60:63], v[188:191]\n"
    "ds_read_b128 a[172:175], v201 offset:29696\n"
    "v_mfma_f32_16x16x32_bf16 v[140:143], a[120:123], a[56:59], v[140:143]\n"
    "v_exp_f32 v128, v128\n"
    "v_mfma_f32_16x16x32_bf16 v[140:143], a[116:119], a[52:55], v[140:143]\n"
    "s_waitcnt lgkmcnt(4)\n"
    "ds_read_b128 a[76:79], v203 offset:3072\n"
    "v_mfma_f32_16x16x32_bf16 v[140:143], a[112:115], a[48:51], v[140:143]\n"
    "v_exp_f32 v129, v129\n"
    "v_smfmac_f32_16x16x64_bf16 a[204:207], a[192:195], v[168:175], v240\n"
    "ds_read_b128 a[176:179], v201 offset:22528\n"
    "v_mfma_f32_16x16x32_bf16 v[64:67], v[168:171], a[128:131], v[64:67]\n"
    "v_exp_f32 v130, v130\n"
    "v_mfma_f32_16x16x32_bf16 v[64:67], v[172:175], a[132:135], v[64:67]\n"
    "ds_read_b128 a[92:95], v203 offset:7168\n"
    "v_cvt_pk_bf16_f32 v160, v128, v129\n"
    "v_mfma_f32_16x16x32_bf16 v[68:71], v[168:171], a[136:139], v[68:71]\n"
    "v_exp_f32 v131, v131\n"
    "v_mfma_f32_16x16x32_bf16 v[68:71], v[172:175], a[140:143], v[68:71]\n"
    "ds_read_b128 a[180:183], v201 offset:30720\n"
    "v_cvt_pk_bf16_f32 v161, v130, v131\n"
    "v_mfma_f32_16x16x32_bf16 v[72:75], v[168:171], a[144:147], v[72:75]\n"
    "v_exp_f32 v132, v132\n"
    "v_accvgpr_read_b32 v196, a204\n"
    "v_accvgpr_read_b32 v197, a205\n"
    "v_add_f32 v196, v196, v197\n"
    "v_mfma_f32_16x16x32_bf16 v[72:75], v[172:175], a[148:151], v[72:75]\n"
    "ds_read_b128 a[88:91], v203 offset:6144\n"
    "v_mfma_f32_16x16x4_f32 v[220:223], v196, v228, 0\n"
    "v_mfma_f32_16x16x32_bf16 v[76:79], v[168:171], a[152:155], v[76:79]\n"
    "v_exp_f32 v133, v133\n"
    "v_mfma_f32_16x16x32_bf16 v[76:79], v[172:175], a[156:159], v[76:79]\n"
    "ds_read_b128 a[184:187], v201 offset:23552\n"
    "v_cvt_pk_bf16_f32 v162, v132, v133\n"
    "v_mfma_f32_16x16x32_bf16 v[80:83], v[168:171], a[160:163], v[80:83]\n"
    "ds_read_b128 a[84:87], v203 offset:5120\n"
    "ds_read_b128 a[188:191], v201 offset:31744\n"
    "v_mfma_f32_16x16x32_bf16 v[80:83], v[172:175], a[164:167], v[80:83]\n"
    "s_waitcnt lgkmcnt(5)\n"
    "v_exp_f32 v134, v134\n"
    "v_mfma_f32_16x16x32_bf16 v[84:87], v[168:171], a[168:171], v[84:87]\n"
    "v_exp_f32 v135, v135\n"
    "v_mfma_f32_16x16x32_bf16 v[84:87], v[172:175], a[172:175], v[84:87]\n"
    "ds_read_b128 a[80:83], v203 offset:4096\n"
    "v_cvt_pk_bf16_f32 v163, v134, v135\n"
    "v_mfma_f32_16x16x32_bf16 v[88:91], v[168:171], a[176:179], v[88:91]\n"
    "s_waitcnt lgkmcnt(5)\n"
    "v_exp_f32 v136, v136\n"
    "v_mfma_f32_16x16x32_bf16 v[88:91], v[172:175], a[180:183], v[88:91]\n"
    "s_waitcnt lgkmcnt(3)\n"
    "ds_read_b128 a[96:99], v203 offset:8192\n"
    "v_mfma_f32_16x16x32_bf16 v[92:95], v[168:171], a[184:187], v[92:95]\n"
    "s_waitcnt lgkmcnt(2)\n"
    "v_exp_f32 v137, v137\n"
    "v_rcp_f32 v220, v220\n"
    "v_mfma_f32_16x16x32_bf16 v[92:95], v[172:175], a[188:191], v[92:95]\n"
    "ds_read_b128 a[100:103], v203 offset:9216\n"
    "v_cvt_pk_bf16_f32 v164, v136, v137\n"
    "v_rcp_f32 v221, v221\n"
    "v_mfma_f32_16x16x32_bf16 v[144:147], a[64:67], a[48:51], v[188:191]\n"
    "v_exp_f32 v138, v138\n"
    "v_mfma_f32_16x16x32_bf16 v[144:147], a[68:71], a[52:55], v[144:147]\n"
    "ds_read_b128 a[104:107], v203 offset:10240\n"
    "v_xor_b32 v250, 0x3000, v250\n"
    "v_mfma_f32_16x16x32_bf16 v[144:147], a[72:75], a[56:59], v[144:147]\n"
    "v_exp_f32 v139, v139\n"
    "v_rcp_f32 v222, v222\n"
    "v_rcp_f32 v223, v223\n"
    "v_pk_mul_f32 v[64:65], v[64:65], v[220:221]\n"
    "v_pk_mul_f32 v[68:69], v[68:69], v[220:221]\n"
    "v_pk_mul_f32 v[72:73], v[72:73], v[220:221]\n"
    "v_pk_mul_f32 v[76:77], v[76:77], v[220:221]\n"
    "v_pk_mul_f32 v[80:81], v[80:81], v[220:221]\n"
    "v_pk_mul_f32 v[84:85], v[84:85], v[220:221]\n"
    "v_pk_mul_f32 v[88:89], v[88:89], v[220:221]\n"
    "v_pk_mul_f32 v[92:93], v[92:93], v[220:221]\n"
    "v_pk_mul_f32 v[66:67], v[66:67], v[222:223]\n"
    "v_pk_mul_f32 v[70:71], v[70:71], v[222:223]\n"
    "v_pk_mul_f32 v[74:75], v[74:75], v[222:223]\n"
    "v_pk_mul_f32 v[78:79], v[78:79], v[222:223]\n"
    "v_pk_mul_f32 v[82:83], v[82:83], v[222:223]\n"
    "v_pk_mul_f32 v[86:87], v[86:87], v[222:223]\n"
    "v_pk_mul_f32 v[90:91], v[90:91], v[222:223]\n"
    "v_pk_mul_f32 v[94:95], v[94:95], v[222:223]\n"
    "v_mfma_f32_16x16x32_bf16 v[144:147], a[76:79], a[60:63], v[144:147]\n"
    "ds_read_b128 a[108:111], v203 offset:11264\n"
    "v_cvt_pk_bf16_f32 v165, v138, v139\n"
    "v_cvt_pk_bf16_f32 v204, v64, v68\n"
    "v_mfma_f32_16x16x32_bf16 v[148:151], a[92:95], a[60:63], v[188:191]\n"
    "v_exp_f32 v140, v140\n"
    "v_cvt_pk_bf16_f32 v205, v72, v76\n"
    "v_mfma_f32_16x16x32_bf16 v[148:151], a[88:91], a[56:59], v[148:151]\n"
    "ds_read_b128 a[124:127], v203 offset:15360\n"
    "v_cvt_pk_bf16_f32 v206, v80, v84\n"
    "v_mfma_f32_16x16x32_bf16 v[148:151], a[84:87], a[52:55], v[148:151]\n"
    "s_waitcnt lgkmcnt(4)\n"
    "ds_read_b128 a[120:123], v203 offset:14336\n"
    "v_cvt_pk_bf16_f32 v207, v88, v92\n"
    "v_mfma_f32_16x16x32_bf16 v[148:151], a[80:83], a[48:51], v[148:151]\n"
    "global_store_dwordx4 v250, v[204:207], s[46:47] offset:0 sc1 nt\n"
    "ds_read_b128 a[116:119], v203 offset:13312\n"
    "207: s_cbranch_execz 199b\n"
    "v_mfma_f32_16x16x32_bf16 v[152:155], a[96:99], a[48:51], v[188:191]\n"
    "s_waitcnt lgkmcnt(5)\n"
    "ds_read_b128 a[112:115], v203 offset:12288\n"
    "v_cvt_pk_bf16_f32 v208, v65, v69\n"
    "v_mfma_f32_16x16x32_bf16 v[152:155], a[100:103], a[52:55], v[152:155]\n"
    "s_waitcnt lgkmcnt(5)\n"
    "v_exp_f32 v141, v141\n"
    "v_mfma_f32_16x16x32_bf16 v[152:155], a[104:107], a[56:59], v[152:155]\n"
    "s_waitcnt lgkmcnt(4)\n"
    "v_exp_f32 v142, v142\n"
    "v_cvt_pk_bf16_f32 v209, v73, v77\n"
    "v_mfma_f32_16x16x32_bf16 v[152:155], a[108:111], a[60:63], v[152:155]\n"
    "s_waitcnt lgkmcnt(3)\n"
    "v_exp_f32 v143, v143\n"
    "v_mfma_f32_16x16x32_bf16 v[156:159], a[124:127], a[60:63], v[188:191]\n"
    "s_waitcnt lgkmcnt(2)\n"
    "v_exp_f32 v144, v144\n"
    "v_cvt_pk_bf16_f32 v210, v81, v85\n"
    "v_mfma_f32_16x16x32_bf16 v[156:159], a[120:123], a[56:59], v[156:159]\n"
    "s_waitcnt lgkmcnt(1)\n"
    "v_exp_f32 v145, v145\n"
    "v_cvt_pk_bf16_f32 v211, v89, v93\n"
    "v_mfma_f32_16x16x32_bf16 v[156:159], a[116:119], a[52:55], v[156:159]\n"
    "global_store_dwordx4 v250, v[208:211], s[46:47] offset:256 sc1 nt\n"
    "v_cvt_pk_bf16_f32 v166, v140, v141\n"
    "v_cvt_pk_bf16_f32 v167, v142, v143\n"
    "v_cvt_pk_bf16_f32 v168, v144, v145\n"
    "v_exp_f32 v146, v146\n"
    "s_waitcnt lgkmcnt(0)\n"
    "v_mfma_f32_16x16x32_bf16 v[156:159], a[112:115], a[48:51], v[156:159]\n"
    "v_exp_f32 v147, v147\n"
    "v_cvt_pk_bf16_f32 v212, v66, v70\n"
    "v_mfma_f32_16x16x32_bf16 v[96:99], v[160:163], a[128:131], v[96:99]\n"
    "v_exp_f32 v148, v148\n"
    "v_cvt_pk_bf16_f32 v213, v74, v78\n"
    "v_mfma_f32_16x16x32_bf16 v[96:99], v[164:167], a[132:135], v[96:99]\n"
    "v_exp_f32 v149, v149\n"
    "v_mfma_f32_16x16x32_bf16 v[100:103], v[160:163], a[136:139], v[100:103]\n"
    "v_exp_f32 v150, v150\n"
    "v_cvt_pk_bf16_f32 v214, v82, v86\n"
    "v_mfma_f32_16x16x32_bf16 v[100:103], v[164:167], a[140:143], v[100:103]\n"
    "v_exp_f32 v151, v151\n"
    "v_cvt_pk_bf16_f32 v215, v90, v94\n"
    "v_mfma_f32_16x16x32_bf16 v[104:107], v[160:163], a[144:147], v[104:107]\n"
    "global_store_dwordx4 v250, v[212:215], s[46:47] offset:512 sc1 nt\n"
    "v_cvt_pk_bf16_f32 v169, v146, v147\n"
    "v_mfma_f32_16x16x32_bf16 v[104:107], v[164:167], a[148:151], v[104:107]\n"
    "v_exp_f32 v152, v152\n"
    "v_cvt_pk_bf16_f32 v216, v67, v71\n"
    "v_mfma_f32_16x16x32_bf16 v[108:111], v[160:163], a[152:155], v[108:111]\n"
    "v_exp_f32 v153, v153\n"
    "v_cvt_pk_bf16_f32 v217, v75, v79\n"
    "v_mfma_f32_16x16x32_bf16 v[108:111], v[164:167], a[156:159], v[108:111]\n"
    "v_exp_f32 v154, v154\n"
    "v_mfma_f32_16x16x32_bf16 v[112:115], v[160:163], a[160:163], v[112:115]\n"
    "v_exp_f32 v155, v155\n"
    "v_cvt_pk_bf16_f32 v218, v83, v87\n"
    "v_mfma_f32_16x16x32_bf16 v[112:115], v[164:167], a[164:167], v[112:115]\n"
    "v_exp_f32 v156, v156\n"
    "v_cvt_pk_bf16_f32 v219, v91, v95\n"
    "v_mfma_f32_16x16x32_bf16 v[116:119], v[160:163], a[168:171], v[116:119]\n"
    "global_store_dwordx4 v250, v[216:219], s[46:47] offset:768 sc1 nt\n"
    "v_cvt_pk_bf16_f32 v170, v148, v149\n"
    "v_cvt_pk_bf16_f32 v171, v150, v151\n"
    "v_mfma_f32_16x16x32_bf16 v[116:119], v[164:167], a[172:175], v[116:119]\n"
    "ds_read_b128 a[128:131], v203 offset:16384\n"
    "v_cvt_pk_bf16_f32 v172, v152, v153\n"
    "v_cvt_pk_bf16_f32 v173, v154, v155\n"
    "v_mfma_f32_16x16x32_bf16 v[120:123], v[160:163], a[176:179], v[120:123]\n"
    "ds_read_b128 a[132:135], v203 offset:24576\n"
    "v_exp_f32 v157, v157\n"
    "v_mfma_f32_16x16x32_bf16 v[120:123], v[164:167], a[180:183], v[120:123]\n"
    "ds_read_b128 a[136:139], v203 offset:17408\n"
    "v_cvt_pk_bf16_f32 v174, v156, v157\n"
    "v_exp_f32 v158, v158\n"
    "v_mfma_f32_16x16x32_bf16 v[124:127], v[160:163], a[184:187], v[124:127]\n"
    "ds_read_b128 a[140:143], v203 offset:25600\n"
    "v_exp_f32 v159, v159\n"
    "v_mfma_f32_16x16x32_bf16 v[124:127], v[164:167], a[188:191], v[124:127]\n"
    "ds_read_b128 a[144:147], v203 offset:18432\n"
    "v_cvt_pk_bf16_f32 v175, v158, v159\n"
    "v_smfmac_f32_16x16x64_bf16 a[208:211], a[192:195], v[160:167], v240\n"
    "ds_read_b128 a[148:151], v203 offset:26624\n"
    "v_smfmac_f32_16x16x64_bf16 a[208:211], a[192:195], v[168:175], v240\n"
    "s_waitcnt lgkmcnt(5)\n"
    "ds_read_b128 a[152:155], v203 offset:19456\n"
    "v_mfma_f32_16x16x32_bf16 v[96:99], v[168:171], a[128:131], v[96:99]\n"
    "s_waitcnt lgkmcnt(5)\n"
    "ds_read_b128 a[156:159], v203 offset:27648\n"
    "v_mfma_f32_16x16x32_bf16 v[96:99], v[172:175], a[132:135], v[96:99]\n"
    "s_waitcnt lgkmcnt(5)\n"
    "ds_read_b128 a[160:163], v203 offset:20480\n"
    "v_mfma_f32_16x16x32_bf16 v[100:103], v[168:171], a[136:139], v[100:103]\n"
    "s_waitcnt lgkmcnt(5)\n"
    "ds_read_b128 a[164:167], v203 offset:28672\n"
    "v_mfma_f32_16x16x32_bf16 v[100:103], v[172:175], a[140:143], v[100:103]\n"
    "s_waitcnt lgkmcnt(5)\n"
    "ds_read_b128 a[168:171], v203 offset:21504\n"
    "v_mfma_f32_16x16x32_bf16 v[104:107], v[168:171], a[144:147], v[104:107]\n"
    "ds_read_b128 a[172:175], v203 offset:29696\n"
    "v_accvgpr_read_b32 v198, a208\n"
    "v_accvgpr_read_b32 v199, a209\n"
    "v_add_f32 v198, v198, v199\n"
    "s_waitcnt lgkmcnt(6)\n"
    "v_mfma_f32_16x16x32_bf16 v[104:107], v[172:175], a[148:151], v[104:107]\n"
    "ds_read_b128 a[176:179], v203 offset:22528\n"
    "v_min3_f32 v231, v192, v194, v196\n"
    "v_mfma_f32_16x16x4_f32 v[224:227], v198, v228, 0\n"
    "s_waitcnt lgkmcnt(6)\n"
    "v_mfma_f32_16x16x32_bf16 v[108:111], v[168:171], a[152:155], v[108:111]\n"
    "s_waitcnt lgkmcnt(5)\n"
    "ds_read_b128 a[180:183], v203 offset:30720\n"
    "v_min_f32 v231, v231, v198\n"
    "v_mfma_f32_16x16x32_bf16 v[108:111], v[172:175], a[156:159], v[108:111]\n"
    "s_waitcnt lgkmcnt(5)\n"
    "ds_read_b128 a[184:187], v203 offset:23552\n"
    "v_cmp_gt_f32 vcc, 0x27800000, v231\n"
    "v_mfma_f32_16x16x32_bf16 v[112:115], v[168:171], a[160:163], v[112:115]\n"
    "s_waitcnt lgkmcnt(5)\n"
    "ds_read_b128 a[188:191], v203 offset:31744\n"
    "v_add_f32 v229, v192, v194\n"
    "v_mfma_f32_16x16x32_bf16 v[112:115], v[172:175], a[164:167], v[112:115]\n"
    "s_waitcnt lgkmcnt(5)\n"
    "v_rcp_f32 v224, v224\n"
    "v_mfma_f32_16x16x32_bf16 v[116:119], v[168:171], a[168:171], v[116:119]\n"
    "s_waitcnt lgkmcnt(4)\n"
    "v_rcp_f32 v225, v225\n"
    "v_mfma_f32_16x16x32_bf16 v[116:119], v[172:175], a[172:175], v[116:119]\n"
    "s_waitcnt lgkmcnt(3)\n"
    "v_rcp_f32 v226, v226\n"
    "v_mfma_f32_16x16x32_bf16 v[120:123], v[168:171], a[176:179], v[120:123]\n"
    "s_waitcnt lgkmcnt(2)\n"
    "v_rcp_f32 v227, v227\n"
    "v_mfma_f32_16x16x32_bf16 v[120:123], v[172:175], a[180:183], v[120:123]\n"
    "s_waitcnt lgkmcnt(1)\n"
    "v_add_f32 v230, v196, v198\n"
    "v_or3_b32 v232, v232, v233, v234\n"
    "v_mfma_f32_16x16x32_bf16 v[124:127], v[168:171], a[184:187], v[124:127]\n"
    "s_waitcnt lgkmcnt(0)\n"
    "v_add_f32 v229, v229, v230\n"
    "v_or_b32 v232, v232, v235\n"
    "v_mfma_f32_16x16x32_bf16 v[124:127], v[172:175], a[188:191], v[124:127]\n"
    "v_xor_b32 v250, 0x1000, v250\n"
    "s_and_b32 s49, vcc_lo, 0xffff\n"
    "v_cmp_nge_f32 vcc, 0x5d800000, v229\n"
    "v_readfirstlane_b32 s48, v232\n"
    "v_pk_mul_f32 v[96:97], v[96:97], v[224:225]\n"
    "v_pk_mul_f32 v[100:101], v[100:101], v[224:225]\n"
    "v_pk_mul_f32 v[104:105], v[104:105], v[224:225]\n"
    "v_pk_mul_f32 v[108:109], v[108:109], v[224:225]\n"
    "v_pk_mul_f32 v[112:113], v[112:113], v[224:225]\n"
    "v_pk_mul_f32 v[116:117], v[116:117], v[224:225]\n"
    "v_pk_mul_f32 v[120:121], v[120:121], v[224:225]\n"
    "v_pk_mul_f32 v[124:125], v[124:125], v[224:225]\n"
    "v_pk_mul_f32 v[98:99], v[98:99], v[226:227]\n"
    "v_pk_mul_f32 v[102:103], v[102:103], v[226:227]\n"
    "v_pk_mul_f32 v[106:107], v[106:107], v[226:227]\n"
    "v_pk_mul_f32 v[110:111], v[110:111], v[226:227]\n"
    "v_pk_mul_f32 v[114:115], v[114:115], v[226:227]\n"
    "v_pk_mul_f32 v[118:119], v[118:119], v[226:227]\n"
    "v_pk_mul_f32 v[122:123], v[122:123], v[226:227]\n"
    "v_pk_mul_f32 v[126:127], v[126:127], v[226:227]\n"
    "v_cvt_pk_bf16_f32 v204, v96, v100\n"
    "v_cvt_pk_bf16_f32 v205, v104, v108\n"
    "v_cvt_pk_bf16_f32 v206, v112, v116\n"
    "v_cvt_pk_bf16_f32 v207, v120, v124\n"
    "v_cvt_pk_bf16_f32 v208, v97, v101\n"
    "v_cvt_pk_bf16_f32 v209, v105, v109\n"
    "v_cvt_pk_bf16_f32 v210, v113, v117\n"
    "v_cvt_pk_bf16_f32 v211, v121, v125\n"
    "v_cvt_pk_bf16_f32 v212, v98, v102\n"
    "v_cvt_pk_bf16_f32 v213, v106, v110\n"
    "v_cvt_pk_bf16_f32 v214, v114, v118\n"
    "v_cvt_pk_bf16_f32 v215, v122, v126\n"
    "v_cvt_pk_bf16_f32 v216, v99, v103\n"
    "v_cvt_pk_bf16_f32 v217, v107, v111\n"
    "v_cvt_pk_bf16_f32 v218, v115, v119\n"
    "v_cvt_pk_bf16_f32 v219, v123, v127\n"
    "global_store_dwordx4 v250, v[204:207], s[46:47] offset:0 sc1 nt\n"
    "global_store_dwordx4 v250, v[208:211], s[46:47] offset:256 sc1 nt\n"
    "global_store_dwordx4 v250, v[212:215], s[46:47] offset:512 sc1 nt\n"
    "global_store_dwordx4 v250, v[216:219], s[46:47] offset:768 sc1 nt\n"
    "s_or_b32 vcc_lo, vcc_lo, s49\n"
    "s_cmp_lg_u64 vcc, 0\n"
    "s_cselect_b32 %[flag], 2, 0\n"
    "s_or_b32 %[flag], %[flag], s48\n"
    "s_cmp_lg_u32 %[flag], 0\n"
    "s_cbranch_scc1 11f\n"
    "s_endpgm\n"
    "11:\n"
    "s_waitcnt vmcnt(0)\n"
    : [flag] "=&s"(flag), [ka] "+s"(ka)
    : [bid] "s"(b), [w] "s"(w), [lds] "s"((unsigned)(size_t)lds)
    : "memory", "vcc", "scc", "s6", "s7", "s8", "s9", "s10", "s11", "s12", "s13", "s14", "s15", "s16", "s17", "s18", "s19", "s20", "s21", "s22", "s23", "s24", "s25", "s26", "s27", "s28", "s29", "s30", "s31", "s34", "s35", "s36", "s37", "s38", "s39", "s40", "s41", "s42", "s43", "s44", "s45", "s46", "s47", "s48", "s49", "s50", "s51", "s52", "s53", "s54", "s55", "s56", "s57", "s58", "s59", "s60", "s61", "s62", "s63", "s64", "s65", "s66", "s67", "s68", "s69", "s70", "s71", "s72", "s73", "s74", "s75", "s76", "s77", "s78", "s79", "s80", "s81", "s82", "s83", "s84", "s85", "s86", "s87", "s88", "s89", "s90", "s91", "s92", "s93", "s94", "s95", "v0", "v1", "v2", "v3", "v4", "v5", "v6", "v7", "v8", "v9", "v10", "v11", "v12", "v13", "v14", "v15", "v16", "v17", "v18", "v19", "v20", "v21", "v22", "v23", "v24", "v25", "v26", "v27", "v28", "v29", "v30", "v31", "v32", "v33", "v34", "v35", "v36", "v37", "v38", "v39", "v40", "v41", "v42", "v43", "v44", "v45", "v46", "v47", "v48", "v49", "v50", "v51", "v52", "v53", "v54", "v55", "v56", "v57", "v58", "v59", "v60", "v61", "v62", "v63", "v64", "v65", "v66", "v67", "v68", "v69", "v70", "v71", "v72", "v73", "v74", "v75", "v76", "v77", "v78", "v79", "v80", "v81", "v82", "v83", "v84", "v85", "v86", "v87", "v88", "v89", "v90", "v91", "v92", "v93", "v94", "v95", "v96", "v97", "v98", "v99", "v100", "v101", "v102", "v103", "v104", "v105", "v106", "v107", "v108", "v109", "v110", "v111", "v112", "v113", "v114", "v115", "v116", "v117", "v118", "v119", "v120", "v121", "v122", "v123", "v124", "v125", "v126", "v127", "v128", "v129", "v130", "v131", "v132", "v133", "v134", "v135", "v136", "v137", "v138", "v139", "v140", "v141", "v142", "v143", "v144", "v145", "v146", "v147", "v148", "v149", "v150", "v151", "v152", "v153", "v154", "v155", "v156", "v157", "v158", "v159", "v160", "v161", "v162", "v163", "v164", "v165", "v166", "v167", "v168", "v169", "v170", "v171", "v172", "v173", "v174", "v175", "v176", "v177", "v178", "v179", "v180", "v181", "v182", "v183", "v184", "v185", "v186", "v187", "v188", "v189", "v190", "v191", "v192", "v193", "v194", "v195", "v196", "v197", "v198", "v199", "v200", "v201", "v202", "v203", "v204", "v205", "v206", "v207", "v208", "v209", "v210", "v211", "v212", "v213", "v214", "v215", "v216", "v217", "v218", "v219", "v220", "v221", "v222", "v223", "v224", "v225", "v226", "v227", "v228", "v229", "v230", "v231", "v232", "v233", "v234", "v235", "v236", "v237", "v238", "v239", "v240", "v241", "v242", "v243", "v244", "v245", "v246", "v247", "v248", "v249", "v250", "v251", "v252", "v253", "a0", "a1", "a2", "a3", "a4", "a5", "a6", "a7", "a8", "a9", "a10", "a11", "a12", "a13", "a14", "a15", "a16", "a17", "a18", "a19", "a20", "a21", "a22", "a23", "a24", "a25", "a26", "a27", "a28", "a29", "a30", "a31", "a32", "a33", "a34", "a35", "a36", "a37", "a38", "a39", "a40", "a41", "a42", "a43", "a44", "a45", "a46", "a47", "a48", "a49", "a50", "a51", "a52", "a53", "a54", "a55", "a56", "a57", "a58", "a59", "a60", "a61", "a62", "a63", "a64", "a65", "a66", "a67", "a68", "a69", "a70", "a71", "a72", "a73", "a74", "a75", "a76", "a77", "a78", "a79", "a80", "a81", "a82", "a83", "a84", "a85", "a86", "a87", "a88", "a89", "a90", "a91", "a92", "a93", "a94", "a95", "a96", "a97", "a98", "a99", "a100", "a101", "a102", "a103", "a104", "a105", "a106", "a107", "a108", "a109", "a110", "a111", "a112", "a113", "a114", "a115", "a116", "a117", "a118", "a119", "a120", "a121", "a122", "a123", "a124", "a125", "a126", "a127", "a128", "a129", "a130", "a131", "a132", "a133", "a134", "a135", "a136", "a137", "a138", "a139", "a140", "a141", "a142", "a143", "a144", "a145", "a146", "a147", "a148", "a149", "a150", "a151", "a152", "a153", "a154", "a155", "a156", "a157", "a158", "a159", "a160", "a161", "a162", "a163", "a164", "a165", "a166", "a167", "a168", "a169", "a170", "a171", "a172", "a173", "a174", "a175", "a176", "a177", "a178", "a179", "a180", "a181", "a182", "a183", "a184", "a185", "a186", "a187", "a188", "a189", "a190", "a191", "a192", "a193", "a194", "a195", "a196", "a197", "a198", "a199", "a200", "a201", "a202", "a203", "a204", "a205", "a206", "a207", "a208", "a209", "a210", "a211");
  if (flag) {
    const Raw r = {ka->qk, ka->qv, ka->ks, ka->kz, ka->vs, ka->vz};
    const unsigned kvh = b & 7, j = b >> 3;
    const size_t rowoff = ((size_t)kvh * 4 * SQ + j * 256 + w * 64) * 256;
    attn_exact(ka->q + rowoff, r, kvh, ka->out + rowoff);
    if ((flag & 1) && __builtin_amdgcn_mbcnt_hi(~0u, __builtin_amdgcn_mbcnt_lo(~0u, 0u)) == 0)
      __hip_atomic_fetch_add((unsigned long long*)(ka->ws + ((size_t)kvh << 21) + 0x104000), 0x9e3779b97f4a7c15ull, __ATOMIC_RELAXED, __HIP_MEMORY_SCOPE_AGENT);
  }
}

extern "C" void kernel_launch(void* const* d_in, const int* in_sizes, int n_in, void* d_out, int out_size, void* d_ws, size_t ws_size, hipStream_t stream) {
  hipLaunchKernelGGL(attn_main, dim3(256), dim3(256), 0, stream, (const char*)d_in[0], (const int*)d_in[1], (const int*)d_in[2],
      (const unsigned short*)d_in[3], (const unsigned short*)d_in[4], (const unsigned short*)d_in[5], (const unsigned short*)d_in[6], (char*)d_ws, (char*)d_out);
}
```
